```python
import torch

def _softmax(x, axis=-1):
    return torch.softmax(x, dim=axis)
def _gelu(x):
    return torch.nn.functional.gelu(x, approximate="tanh")
def _silu(x):
    return torch.nn.functional.silu(x)
def _sigmoid(x):
    return torch.sigmoid(x)
def _layernorm(x, axis=-1, eps=1e-5):
    m = x.mean(dim=axis, keepdim=True)
    v = x.var(dim=axis, keepdim=True, unbiased=False)
    return (x - m) / torch.sqrt(v + eps)
def _scatter_add(tgt, idx, src, axis=0):
    return tgt.clone().index_add_(axis, idx, src)
def _segment_sum(src, seg, n):
    out = torch.zeros((n,) + src.shape[1:], dtype=src.dtype)
    return out.index_add_(0, seg, src)
def _pad(x, pw):
    flat = tuple(v for pair in reversed(pw) for v in pair)
    return torch.nn.functional.pad(x, flat)

kernel_name = 'standalone__normalfloat_lut_codebook_lookup__prompt'

def setup_inputs():

    _w = (lambda a: (lambda t: t.to(torch.bfloat16) if t.is_floating_point() else t)(torch.as_tensor(a)))
    B, S, D, F = 1, 2048, 1024, 3584
    G = 64 if 64 > 0 else D
    D = (D // G) * G
    ng = D // G
    nf4 = torch.tensor([
        -1.0, -0.6961928009986877, -0.5250730514526367, -0.39491748809814453,
        -0.28444138169288635, -0.18477343022823334, -0.09105003625154495, 0.0,
        0.07958029955625534, 0.16093020141124725, 0.24611230194568634, 0.33791524171829224,
        0.44070982933044434, 0.5626170039176941, 0.7229568362236023, 1.0
    ], dtype=float)
    qw = torch.randint(0, 16, (D, F)).to(torch.int64)
    absmax = (torch.abs((lambda *s: torch.randn(*s, dtype=torch.bfloat16) * 0.05)(ng, F)) + 0.01).to(float)
    return {
        "x": _w((lambda *s: torch.randn(*s, dtype=torch.bfloat16) * 0.05)(B, S, D)),
        "qw": _w(qw),
        "codebook": _w(nf4),
        "absmax": _w(absmax),
    }

def reference(x, qw, codebook, absmax):
    B, S, D = x.shape
    ng, F = absmax.shape
    G = D // ng
    w_nf = codebook[qw]
    s = (lambda x, n, axis: torch.repeat_interleave(x, n, dim=axis))(absmax, G, axis=0)
    w = (lambda x, dt: x.to(dt))(w_nf * s, x.dtype)
    return torch.einsum("bsd,df->bsf", x, w)
```

```cpp
#include <hip/hip_runtime.h>
#include <hip/hip_bf16.h>
#include <stdint.h>

typedef __attribute__((ext_vector_type(8))) short bf16x8;
typedef __attribute__((ext_vector_type(4))) float f32x4;
typedef __attribute__((ext_vector_type(4))) int i32x4;
typedef __attribute__((ext_vector_type(4))) unsigned u32x4;
typedef __attribute__((ext_vector_type(2))) unsigned u32x2;
typedef __attribute__((ext_vector_type(2))) float f32x2;
typedef __attribute__((ext_vector_type(2))) __bf16 bf16x2_t;
typedef __attribute__((address_space(1))) unsigned gu32;

constexpr int M_ = 2048, K_ = 1024, N_ = 3584;
constexpr int BM = 256, BN = 112, BK = 64, NT = K_ / BK;
constexpr int XB = BM * BK * 2;
constexpr int WB = BN * BK * 2;
constexpr int KCB = BN * 16;
constexpr int SB = XB + WB;
constexpr int NS = 3;
constexpr int HDR = 4096;
constexpr int LUT_OFF = -HDR;
constexpr int DUM_OFF = -HDR + 2048;
constexpr int STG_OFF = NS * SB;
constexpr int SHM_BYTES = HDR + NS * SB + WB;
constexpr int EROW = 240;
constexpr size_t WIMG_BYTES = (size_t)32 * NT * WB;
constexpr size_t SYNC_OFF = (size_t)8 << 20;
constexpr unsigned SPIN_MAX = 1u << 17;
constexpr int POLLB_T = 5;
constexpr unsigned CODE_TOUCH_MAX = 11648;
constexpr int AUX_NT = 2, AUX_SC1 = 16;

#define WAIT_V(n) asm volatile("s_waitcnt vmcnt(%0)" ::"n"(n) : "memory")
#define WAIT_L(n) asm volatile("s_waitcnt lgkmcnt(%0)" ::"n"(n) : "memory")
#define BAR() do { asm volatile("" ::: "memory"); __builtin_amdgcn_s_barrier(); asm volatile("" ::: "memory"); } while (0)
#define SCHED() __builtin_amdgcn_sched_barrier(0)
#define SGB(m_, n_) __builtin_amdgcn_sched_group_barrier(m_, n_, 0)
#define LGKM0() do { SCHED(); __builtin_amdgcn_s_waitcnt(0xC07F); SCHED(); } while (0)
#define TAG_OF(e) ((e) * 0x9E3779B1u + 0x7F4A7C15u)
#define RLX_AGENT __ATOMIC_RELAXED, __HIP_MEMORY_SCOPE_AGENT

__device__ __forceinline__ unsigned pack_bf16(float a, float b) {
  f32x2 v = {a, b};
  bf16x2_t r = __builtin_convertvector(v, bf16x2_t);
  return __builtin_bit_cast(unsigned, r);
}
__device__ __forceinline__ int kstep_of(int s, int tm, int tn) {
  const int u = (int)(0xFEDCBA9765432180ull >> (4 * s)) & 15;
  return (u & 8) + ((tm + u + tn) & 7);
}

struct QRegs { i32x4 r[8]; };
struct LV { f32x2 v[4][4]; float sc[4]; };
__device__ __forceinline__ f32x2 pair_lookup(const char* lut, int q0, int q1) { return *(const f32x2*)(lut + (q0 << 3) + (q1 << 7)); }
__device__ __forceinline__ u32x4 q_pack(const LV& l, int i) {
  u32x4 o;
  o.x = pack_bf16(l.v[i][0].x * l.sc[i], l.v[i][0].y * l.sc[i]); o.y = pack_bf16(l.v[i][1].x * l.sc[i], l.v[i][1].y * l.sc[i]);
  o.z = pack_bf16(l.v[i][2].x * l.sc[i], l.v[i][2].y * l.sc[i]); o.w = pack_bf16(l.v[i][3].x * l.sc[i], l.v[i][3].y * l.sc[i]);
  return o;
}

__global__ __launch_bounds__(512, 2) void nf4_gemm(const uint16_t* __restrict__ x, const int* __restrict__ qw,
                                                   const uint16_t* __restrict__ cb, const uint16_t* __restrict__ am,
                                                   char* __restrict__ ws, uint16_t* __restrict__ out) {
  __shared__ __attribute__((aligned(1024))) char shm_raw[SHM_BYTES];
  char* const shm = shm_raw + HDR;
  unsigned long long pc0; asm volatile("s_getpc_b64 %0" : "=s"(pc0));
  unsigned ct;
  asm volatile("global_load_dword %0, %1, %2" : "=&v"(ct) : "v"(min((unsigned)(threadIdx.x * 128u), CODE_TOUCH_MAX)), "s"(pc0) : "memory");
  const int bid = blockIdx.x;
  const int xcd = bid & 7, bi = bid >> 3;
  const int tn = (xcd >> 2) * 16 + (bi & 15), tm = (xcd & 3) * 2 + (bi >> 4);
  const int rot = (tm + tn) & 7;
  const int tid = threadIdx.x, lane = tid & 63;
  const int wid = __builtin_amdgcn_readfirstlane(tid >> 6);
  const bool qact = (lane & 31) < 28;
  const int nc = qact ? (lane & 31) : 27;
  const int kc0 = wid < 4 ? 2 * wid : 4 * (wid & 1);
  const unsigned qvoff = (unsigned)(((kc0 + (lane >> 5)) * 8 * N_ + nc * 4) * 4);
  const unsigned avoff = (unsigned)(nc * 8);
  const unsigned qs0 = (unsigned)(tn * BN * 4);
  { unsigned t0_ = qvoff, t1_ = avoff; asm volatile("" : "+v"(t0_), "+v"(t1_)); }
  asm volatile("" :: "s"(qs0 + (unsigned)rot * (BK * N_ * 4)));
  SCHED();
  asm volatile("" :: "s"(x), "s"(qw), "s"(cb), "s"(am), "s"(ws), "s"(out));
  gu32* flags = (gu32*)(ws + SYNC_OFF + (size_t)tn * 512);
  char* lut = shm + LUT_OFF;
  const __amdgpu_buffer_rsrc_t rsW = __builtin_amdgcn_make_buffer_rsrc((void*)ws, (short)0, (int)WIMG_BYTES, 0x00020000);
  const __amdgpu_buffer_rsrc_t rsQ = __builtin_amdgcn_make_buffer_rsrc((void*)qw, (short)0, K_ * N_ * 4, 0x00020000);
  const uint16_t* am0 = am + tn * BN;
  const unsigned zero = 0;
#define Q_ROW(Q, j, so_) asm volatile("buffer_load_dwordx4 %0, %1, %2, %3 offen nt" : "=&v"(Q.r[j]) : "v"(qvoff), "s"(rsQ), "s"((so_) + (j) * (N_ * 4)) : "memory")
#define Q_ROWS(Q, so_) do { _Pragma("unroll") for (int j_ = 0; j_ < 8; j_++) Q_ROW(Q, j_, so_); } while (0)
#define Q_SC(S, ks) asm volatile("global_load_dwordx2 %0, %1, %2" : "=&v"(S) : "v"(avoff), "s"(am0 + (ks) * N_) : "memory")
#define Q_SO(ks) (qs0 + (unsigned)(ks) * (BK * N_ * 4))
#define TAG_LOAD(T) asm volatile("global_load_dword %0, %1, %2 offset:256 sc1" : "=&v"(T) : "v"(zero), "s"(flags) : "memory")
#define Q_PIN(q) asm volatile("" : "+v"(q.r[0]), "+v"(q.r[1]), "+v"(q.r[2]), "+v"(q.r[3]), "+v"(q.r[4]), "+v"(q.r[5]), "+v"(q.r[6]), "+v"(q.r[7]))
#define SCALES(S) do { asm volatile("" : "+v"(S));                                                                                \
      lv.sc[0] = __builtin_bit_cast(float, S.x << 16); lv.sc[1] = __builtin_bit_cast(float, S.x & 0xffff0000u);                   \
      lv.sc[2] = __builtin_bit_cast(float, S.y << 16); lv.sc[3] = __builtin_bit_cast(float, S.y & 0xffff0000u); } while (0)
#define ROUND(Q, dst) do { Q_PIN(Q);                                                                                              \
      _Pragma("unroll") for (int i_ = 0; i_ < 4; i_++) _Pragma("unroll") for (int a_ = 0; a_ < 4; a_++)                           \
        lv.v[i_][a_] = pair_lookup(lut, Q.r[2 * a_][i_], Q.r[2 * a_ + 1][i_]);                                                    \
      SCHED();                                                                                                                    \
      char* d_ = shm + (qact ? (dst) + io : DUM_OFF);                                                                             \
      _Pragma("unroll") for (int i_ = 0; i_ < 4; i_++) *(u32x4*)(d_ + ((i_ << 4) ^ isw)) = q_pack(lv, i_);                        \
      SCHED(); } while (0)
#define PUBLISH(src, ks, kcp) do { WAIT_L(0); if (lane < 56) {                                                                    \
        const unsigned g_ = (unsigned)((tn * NT + (ks)) * WB + (kcp) * (2 * KCB) + lane * 16); u32x4 pv_[4];                      \
        _Pragma("unroll") for (int i_ = 0; i_ < 4; i_++) pv_[i_] = *(const u32x4*)(shm + (src) + lane * 16 + i_ * 896);           \
        _Pragma("unroll") for (int i_ = 0; i_ < 4; i_++) __builtin_amdgcn_raw_buffer_store_b128(pv_[i_], rsW, g_ + i_ * 896, 0, AUX_SC1); } \
      SCHED(); } while (0)
  LV lv;
  const __amdgpu_buffer_rsrc_t rsO = __builtin_amdgcn_make_buffer_rsrc((void*)out, (short)0, M_ * N_ * 2, 0x00020000);
  unsigned vf, vp; int rf, rp;
#define O_ADDR() do { const int f0 = (tn & 3) * 2, r8 = lane >> 3, cq = lane & 7, cf = f0 + cq, cp = cq < f0 ? cq : cq + 8;        \
      const unsigned so_ = (unsigned)(((tm * BM + (wid & 3) * 64 + (wid < 4 ? 32 : 0) + r8) * N_ + tn * BN) * 2);                 \
      const int sb_ = HDR + SB + (wid & 3) * 8192 + (wid < 4 ? 4096 : 0) + r8 * EROW;                                             \
      vf = so_ + cf * 16; vp = cq < 6 ? so_ + cp * 16 : 0x7fffffffu; rf = sb_ + cf * 16; rp = sb_ + (cq < 6 ? cp : 0) * 16;        \
      asm volatile("" : "+v"(vf), "+v"(vp), "+v"(rf), "+v"(rp)); } while (0)
#define O_RD(i_, r_) (*(const u32x4*)(shm_raw + ((i_) < 2 ? rf : rp) + (r_) * SB + ((i_) & 1) * 8 * EROW))
#define O_ST(v_, r_, pa) do {                                                                                                     \
      __builtin_amdgcn_raw_buffer_store_b128(v_[0], rsO, vf + (2 * (r_)) * 8 * (N_ * 2), 0, AUX_NT);                              \
      __builtin_amdgcn_raw_buffer_store_b128(v_[1], rsO, vf + (2 * (r_) + 1) * 8 * (N_ * 2), 0, AUX_NT);                          \
      __builtin_amdgcn_raw_buffer_store_b128(v_[2], rsO, vp + (2 * (r_)) * 8 * (N_ * 2), 0, pa);                                  \
      __builtin_amdgcn_raw_buffer_store_b128(v_[3], rsO, vp + (2 * (r_) + 1) * 8 * (N_ * 2), 0, pa); } while (0)

  if (__builtin_expect(wid < 4, 1)) {
    QRegs q; u32x2 qsv; unsigned c0, c1, tagv;
    asm volatile("global_load_ushort %0, %2, %4\n\tglobal_load_ushort %1, %3, %4" : "=&v"(c0), "=&v"(c1)
                 : "v"((unsigned)((lane & 15) * 2)), "v"((unsigned)((wid * 4 + (lane >> 4)) * 2)), "s"(cb) : "memory");
    Q_ROWS(q, Q_SO(rot));
    Q_SC(qsv, rot);
    TAG_LOAD(tagv);
    SCHED();
    BAR();
    const int p0 = nc < 24 ? 32 * (nc >> 3) + 16 * (nc & 1) + 4 * ((nc & 7) >> 1) : nc * 4;
    const int io = (lane >> 5) * KCB + p0 * 16, isw = ((p0 >> 3) & 3) << 4;
    f32x4 acc[4][7];
#pragma unroll
    for (int m = 0; m < 4; m++)
#pragma unroll
      for (int n = 0; n < 7; n++) { acc[m][n] = (f32x4){0.f, 0.f, 0.f, 0.f}; asm volatile("" : "+v"(acc[m][n])); }
    WAIT_V(10);
    asm volatile("" : "+v"(c0), "+v"(c1)); asm volatile("" :: "v"(ct));
    *(u32x2*)(lut + tid * 8) = (u32x2){c0 << 16, c1 << 16};
    WAIT_L(0);
    BAR();
    WAIT_V(0);
    SCALES(qsv);
    ROUND(q, XB + kc0 * KCB);
    PUBLISH(XB + kc0 * KCB, rot, wid);
    BAR();
    const int fr = lane & 15, fq = lane >> 4;
    const int xo0 = (wid * 64 + fr) * 128 + ((fq ^ (fr >> 1)) << 4), xo1 = xo0 ^ 64;
    const int woe = XB + fq * KCB + ((fr ^ (fr >> 3)) << 4), woo = woe ^ 32;
    bf16x8 xa[4], wa[7], xb[4], wb[7];
#define RD(xf, wf, base, wbase_, xo, hf) do {                                                         \
      _Pragma("unroll") for (int n_ = 0; n_ < 7; n_++) wf[n_] = *(const bf16x8*)((wbase_) + ((n_ & 1) ? woo : woe) + (hf) * (4 * KCB) + n_ * 256); \
      _Pragma("unroll") for (int m_ = 0; m_ < 4; m_++) xf[m_] = *(const bf16x8*)((base) + (xo) + m_ * 2048); } while (0)
#define MMR(xf, wf, i0, i1) do { _Pragma("unroll") for (int i_ = (i0); i_ < (i1); i_++)               \
      acc[i_ / 7][i_ % 7] = __builtin_amdgcn_mfma_f32_16x16x32_bf16(wf[i_ % 7], xf[i_ / 7], acc[i_ / 7][i_ % 7], 0, 0, 0); } while (0)
    O_ADDR();
    int e0 = HDR + SB + wid * 8192 + fr * EROW + fq * 16, e1 = e0 - fq * 8 + 192;
    asm volatile("" : "+v"(e0), "+v"(e1));
    WAIT_V(0);
    asm volatile("" : "+v"(tagv));
    if (lane == 0) __hip_atomic_store(flags + rot * 4 + wid, TAG_OF(tagv), RLX_AGENT);
    BAR();
#pragma unroll 1
    for (int ks = 0; ks < 2; ks++) {
      RD(xa, wa, shm, shm + ks * (4 * KCB), xo0 ^ (ks * 64), 0);
      MMR(xa, wa, 0, 28);
      SCHED();
    }
    BAR();
    RD(xa, wa, shm + SB, shm + (STG_OFF - XB), xo0, 0);
    int ae = woe + (STG_OFF - XB), ao = woo + (STG_OFF - XB), a0 = xo0 + SB, a1 = xo1 + SB, so = SB;
#define RDA(xf, wf, ax, hf) do {                                                                      \
      _Pragma("unroll") for (int n_ = 0; n_ < 7; n_++) wf[n_] = *(const bf16x8*)(shm + ((n_ & 1) ? ao : ae) + (hf) * (4 * KCB) + n_ * 256); \
      _Pragma("unroll") for (int m_ = 0; m_ < 4; m_++) xf[m_] = *(const bf16x8*)(shm + (ax) + m_ * 2048); } while (0)
#define INTER11() do { _Pragma("unroll") for (int i_ = 0; i_ < 11; i_++) { SGB(0x8, 2); SGB(0x100, 1); } } while (0)
    asm volatile("" : "+v"(ae), "+v"(ao), "+v"(a1));
#pragma unroll 1
    for (int t = 1; t < NT - 3; t++) {
      SCHED();
      RDA(xb, wb, a1, 1);
      MMR(xa, wa, 0, 27);
      so = so == 2 * SB ? 0 : so + SB;
      ae = woe + so; ao = woo + so; a0 = xo0 + so; a1 = xo1 + so;
      INTER11(); SGB(0x8, 1); SGB(0x4, 3); SGB(0x8, 1); SGB(0x2, 2); SGB(0x8, 1); SGB(0x2, 2); SGB(0x8, 2);
      asm volatile("" : "+v"(ae), "+v"(ao), "+v"(a0), "+v"(a1));
      LGKM0();
      MMR(xa, wa, 27, 28);
      SCHED();
      BAR();
      SCHED();
      RDA(xa, wa, a0, 0);
      MMR(xb, wb, 0, 28);
      INTER11(); SGB(0x8, 4); SGB(0x4, 2); SGB(0x8, 2);
    }
    SCHED();
#define LD(o_) (*(const bf16x8*)(shm + (o_)))
#define MF(m_, n_, xv, wf) do { acc[m_][n_] = __builtin_amdgcn_mfma_f32_16x16x32_bf16(wf[n_], xv, acc[m_][n_], 0, 0, 0); SCHED(); } while (0)
#define RJ(j_, xf, wf, be, bo, bx, c_, hf, m0) do { if ((j_) == 0) xf[0] = LD((bx) + (c_) + (m0) * 2048);                         \
      else if ((j_) < 8) wf[(j_) - 1] = LD((((j_) & 1) ? (be) : (bo)) + (c_) + (hf) * (4 * KCB) + ((j_) - 1) * 256);              \
      else if ((j_) == 8) xf[1] = LD((bx) + (c_) + ((m0) + 1) * 2048); } while (0)
#define BLK(m0, xc, wc, RD_, FILL) do { _Pragma("unroll") for (int g = 0; g < 14; g++) {                                          \
      MF((m0) + g / 7, g % 7, xc[g / 7], wc); RD_; FILL; SCHED(); } } while (0)
    unsigned pk[2][14];
#define CV(m_, j_) do { if ((j_) >= 0 && (j_) < 14) pk[(m_) & 1][j_] = pack_bf16(acc[m_][(j_) / 2][((j_) & 1) * 2], acc[m_][(j_) / 2][((j_) & 1) * 2 + 1]); } while (0)
#define WR(m_, w_) do { const unsigned* p_ = pk[(m_) & 1]; const int c_ = ((m_) >> 1) * 4096 + ((m_) & 1) * SB;                    \
      if ((w_) >= 0 && (w_) < 3) *(u32x4*)(shm_raw + e0 + c_ + (w_) * 64) = (u32x4){p_[4 * ((w_) & 3)], p_[4 * ((w_) & 3) + 1], p_[(4 * (w_) + 2) % 14], p_[(4 * (w_) + 3) % 14]}; \
      else if ((w_) == 3) *(u32x2*)(shm_raw + e1 + c_) = (u32x2){p_[12], p_[13]}; } while (0)
    {
#pragma unroll
      for (int g = 0; g < 28; g++) { MF(g / 7, g % 7, xa[g / 7], wa); if (g & 1) RJ(g / 2, xb, wb, ae, ao, a1, 0, 1, 0); SCHED(); }
      BAR();
      SCHED();
      BLK(0, xb, wb, RJ(g, xa, wa, ae, ao, a0, SB, 0, 0), );
      BLK(0, xa, wa, RJ(g, xb, wb, ae, ao, a1, SB, 1, 0), );
      BAR();
      SCHED();
      BLK(0, xb, wb, RJ(g, xa, wa, woe, woo, xo0, 0, 0, 0), );
      BLK(0, xa, wa, RJ(g, xb, wb, woe, woo, xo1, 0, 1, 0), );
      BLK(0, xb, wb, RJ(g, xa, wa, ae, ao, a1, 0, 1, 2), if (g >= 9) { CV(0, 2 * g - 18); CV(0, 2 * g - 17); });
      BLK(2, xa, wa, RJ(g, xb, wb, ae, ao, a0, SB, 0, 2),
          if (g < 4) CV(0, g + 10); else if (g < 9) CV(1, g - 4); else if (g == 13) { CV(1, 9); CV(1, 10); } else { CV(1, g - 4); WR(0, g - 9); });
#pragma unroll
      for (int g = 0; g < 14; g++) {
        MF(2 + g / 7, g % 7, xb[g / 7], wb);
        if (g < 3) CV(1, g + 11);
        if (g < 4) WR(1, g); else RJ(g - 4, xa, wa, ae, ao, a1, SB, 1, 2);
        SCHED();
        if (g == 8) {
          WAIT_L(5);
          BAR();
          SCHED();
        }
      }
      BLK(2, xa, wa, RJ(g, xb, wb, woe, woo, xo0, 0, 0, 2), );
#pragma unroll
      for (int g = 0; g < 10; g++) { MF(2 + g / 7, g % 7, xb[g / 7], wb); RJ(g, xa, wa, woe, woo, xo1, 0, 1, 2); SCHED(); }
#pragma unroll
      for (int g = 0; g < 7; g++) { MF(2, g, xa[0], wa); if (g >= 3) { CV(2, 2 * g - 6); CV(2, 2 * g - 5); } SCHED(); }
      u32x4 ov[4];
#pragma unroll
      for (int g = 0; g < 11; g++) {
        if (g < 4) MF(3, g + 3, xb[1], wb); else MF(3, g - 4, xa[1], wa);
        if (g < 2) { CV(2, 8 + g); WR(2, g); } else if (g < 4) { CV(2, 2 * g + 6); CV(2, 2 * g + 7); } else if (g < 6) { WR(2, g - 2); }
        else if (g < 8) { ov[2 * g - 12] = O_RD(2 * g - 12, 0); ov[2 * g - 11] = O_RD(2 * g - 11, 0); }
        else { CV(3, 2 * g - 16); CV(3, 2 * g - 15); }
        SCHED();
      }
      O_ST(ov, 0, 0);
      SCHED();
#pragma unroll
      for (int j = 6; j < 14; j++) { CV(3, j); if (j == 7) { WR(3, 0); WR(3, 1); } if (j == 11) WR(3, 2); SCHED(); }
      WR(3, 3);
      SCHED();
    }
  } else if (wid < 6) {
    const int h = wid - 4;
    unsigned xv[16];
#pragma unroll
    for (int i = 0; i < 16; i++) xv[i] = (unsigned)((8 * i + (lane >> 3)) * (K_ * 2) + (((lane & 7) ^ ((i & 1) * 4 + (lane >> 4))) << 4));
    const char* xbase = (const char*)(x + (size_t)(tm * BM + h * 128) * K_);
    const unsigned ldsx = (unsigned)(size_t)(shm) + h * 16384;
#define X8(o, l_, b_)                                                                                                             \
      asm volatile("s_mov_b32 m0, %8\n\ts_nop 0\n\tglobal_load_lds_dwordx4 %0, %9\n\t"                                            \
                   "s_add_u32 m0, m0, 0x400\n\ts_nop 0\n\tglobal_load_lds_dwordx4 %1, %9\n\t"                                     \
                   "s_add_u32 m0, m0, 0x400\n\ts_nop 0\n\tglobal_load_lds_dwordx4 %2, %9\n\t"                                     \
                   "s_add_u32 m0, m0, 0x400\n\ts_nop 0\n\tglobal_load_lds_dwordx4 %3, %9\n\t"                                     \
                   "s_add_u32 m0, m0, 0x400\n\ts_nop 0\n\tglobal_load_lds_dwordx4 %4, %9\n\t"                                     \
                   "s_add_u32 m0, m0, 0x400\n\ts_nop 0\n\tglobal_load_lds_dwordx4 %5, %9\n\t"                                     \
                   "s_add_u32 m0, m0, 0x400\n\ts_nop 0\n\tglobal_load_lds_dwordx4 %6, %9\n\t"                                     \
                   "s_add_u32 m0, m0, 0x400\n\ts_nop 0\n\tglobal_load_lds_dwordx4 %7, %9"                                         \
                   :: "v"(xv[o]), "v"(xv[o + 1]), "v"(xv[o + 2]), "v"(xv[o + 3]), "v"(xv[o + 4]), "v"(xv[o + 5]), "v"(xv[o + 6]), "v"(xv[o + 7]), \
                      "s"(l_), "s"(b_) : "memory", "scc")
#define ISSUE_X(s, slot) do { const char* b_ = xbase + kstep_of(s, tm, tn) * 128; const unsigned l_ = ldsx + (slot) * SB;       \
      X8(0, l_, b_); X8(8, l_ + 8192, b_); } while (0)
    BAR();
    BAR();
    BAR();
    ISSUE_X(0, 0);
    ISSUE_X(1, 1);
    WAIT_V(16);
    asm volatile("" :: "v"(ct));
    BAR();
    int xslot = 2;
#pragma unroll 1
    for (int t = -1; t < NT - 2; t++) {
      if (t + 3 < NT) { ISSUE_X(t + 3, xslot); xslot = xslot == NS - 1 ? 0 : xslot + 1; WAIT_V(16); } else { WAIT_V(0); }
      BAR();
    }
  } else {
    QRegs qa, qb; u32x2 sb; unsigned tagv;
    constexpr unsigned RO = 16 * N_ * 4;
    Q_SC(sb, rot + 8); TAG_LOAD(tagv);
    const int p0 = nc < 24 ? 32 * (nc >> 3) + 16 * (nc & 1) + 4 * ((nc & 7) >> 1) : nc * 4;
    const int io = (lane >> 5) * KCB + p0 * 16, isw = ((p0 >> 3) & 3) << 4;
    BAR();
    Q_ROWS(qa, Q_SO(rot + 8));
    Q_ROWS(qb, Q_SO(rot + 8) + RO);
    BAR();
    BAR();
    WAIT_V(8);
    asm volatile("" :: "v"(ct));
    SCALES(sb);
    ROUND(qa, STG_OFF + kc0 * KCB);
    WAIT_V(0);
    ROUND(qb, STG_OFF + (kc0 + 2) * KCB);
    WAIT_L(0);
    asm volatile("" : "+v"(tagv));
    const unsigned tag = TAG_OF(tagv);
   if (wid == 6) {
    const char* wbase = ws + (size_t)tn * NT * WB;
    const unsigned wv = (unsigned)(lane * 16), wv2 = wv + 4096, wv3 = wv + 8192, wv4 = wv + 12288;
    const unsigned ldsw = (unsigned)(size_t)(shm) + XB;
#define ISSUE_W(s, slot) do { const char* b_ = wbase + (size_t)kstep_of(s, tm, tn) * WB; const unsigned l_ = ldsw + (slot) * SB; \
      asm volatile("s_mov_b32 m0, %4\n\ts_nop 0\n\t"                                                                              \
                   "global_load_lds_dwordx4 %0, %5 sc1\n\tglobal_load_lds_dwordx4 %0, %5 offset:1024 sc1\n\t"                     \
                   "global_load_lds_dwordx4 %0, %5 offset:2048 sc1\n\tglobal_load_lds_dwordx4 %0, %5 offset:3072 sc1\n\t"         \
                   "s_add_u32 m0, m0, 0x1000\n\ts_nop 0\n\t"                                                                      \
                   "global_load_lds_dwordx4 %1, %5 sc1\n\tglobal_load_lds_dwordx4 %1, %5 offset:1024 sc1\n\t"                     \
                   "global_load_lds_dwordx4 %1, %5 offset:2048 sc1\n\tglobal_load_lds_dwordx4 %1, %5 offset:3072 sc1\n\t"         \
                   "s_add_u32 m0, m0, 0x1000\n\ts_nop 0\n\t"                                                                      \
                   "global_load_lds_dwordx4 %2, %5 sc1\n\tglobal_load_lds_dwordx4 %2, %5 offset:1024 sc1\n\t"                     \
                   "global_load_lds_dwordx4 %2, %5 offset:2048 sc1\n\tglobal_load_lds_dwordx4 %2, %5 offset:3072 sc1\n\t"         \
                   "s_add_u32 m0, m0, 0x1000\n\ts_nop 0\n\t"                                                                      \
                   "global_load_lds_dwordx4 %3, %5 sc1\n\tglobal_load_lds_dwordx4 %3, %5 offset:1024 sc1"                         \
                   :: "v"(wv), "v"(wv2), "v"(wv3), "v"(wv4), "s"(l_), "s"(b_) : "memory", "scc"); } while (0)
    const unsigned fvo = (unsigned)(lane * 4);
#define SET_RDY(f_) do { const unsigned long long b_ = __ballot((f_) == tag);                                                     \
      rda = (unsigned)__builtin_amdgcn_readfirstlane((unsigned)b_); rdb = (unsigned)__builtin_amdgcn_readfirstlane((unsigned)(b_ >> 32)); } while (0)
#define POLL_BLOCK() do { unsigned f_; asm volatile("global_load_dword %0, %1, %2 sc1\n\ts_waitcnt vmcnt(0)" : "=&v"(f_) : "v"(fvo), "s"(flags) : "memory"); \
      SET_RDY(f_); } while (0)
#define READY(s) (((((s) > 8 ? rdb : rda) >> ((kstep_of(s, tm, tn) & 7) * 4)) & 15u) == 15u)
#define NEXT_W() do { nw++; wslot = wslot == NS - 1 ? 0 : wslot + 1; } while (0)
    unsigned rda = 0, rdb = 0, spins = 0;
    BAR();
    int nw = 2, wslot = 2;
    unsigned fv = tagv;
    bool ppoll = false, preg = false;
#pragma unroll 1
    for (int t = -1; t < NT - 2; t++) {
      const int lim = t + 3 < NT ? t + 3 : NT - 1;
      if (preg) { WAIT_V(14); } else { WAIT_V(0); }
      asm volatile("" : "+v"(fv));
      if (ppoll) SET_RDY(fv);
      if (nw <= t + 2) {
#pragma unroll 1
        while (!READY(nw)) { if (++spins > SPIN_MAX) { rda = ~0u; rdb = ~0u; break; } POLL_BLOCK(); }
        ISSUE_W(nw, wslot); NEXT_W();
      }
      ppoll = spins <= SPIN_MAX && (t < POLLB_T ? rda != ~0u : rdb != ~0u);
      asm volatile("global_load_dword %0, %1, %2 sc1" : "+v"(fv) : "v"(fvo), "s"(ppoll ? (const char*)flags : (const char*)am) : "memory");
      preg = nw <= lim && READY(nw);
      if (preg) { ISSUE_W(nw, wslot); NEXT_W(); WAIT_V(15); } else { WAIT_V(1); }
      BAR();
    }
    WAIT_V(0);
    asm volatile("" : "+v"(fv));
    if (tm == 0) {
#pragma unroll 1
      while ((rda & rdb) != ~0u && ++spins < SPIN_MAX) POLL_BLOCK();
      if (lane == 0) __hip_atomic_store(flags + 64, tag, RLX_AGENT);
    }
   } else {
    BAR();
    BAR();
#pragma unroll 1
    for (int i = 0; i < 2; i++) {
      PUBLISH(STG_OFF + 4 * i * KCB, rot + 8, 2 * i);
      PUBLISH(STG_OFF + (4 * i + 2) * KCB, rot + 8, 2 * i + 1);
      BAR();
    }
    BAR();
    WAIT_V(0);
    if (lane < 4) __hip_atomic_store(flags + (rot + 8) * 4 + lane, tag, RLX_AGENT);
#pragma unroll 1
    for (int t = 4; t < NT - 1; t++) BAR();
   }
  }
  {
    u32x4 ob[4];
    if (wid >= 4) {
      O_ADDR();
      BAR();
#pragma unroll
      for (int i = 0; i < 4; i++) ob[i] = O_RD(i, 0);
      O_ST(ob, 0, 0);
    }
#pragma unroll
    for (int i = 0; i < 4; i++) ob[i] = O_RD(i, 1);
    O_ST(ob, 1, 0);
  }
}

extern "C" void kernel_launch(void* const* d_in, const int* in_sizes, int n_in, void* d_out, int out_size, void* d_ws,
                              size_t ws_size, hipStream_t stream) {
  nf4_gemm<<<256, 512, 0, stream>>>((const uint16_t*)d_in[0], (const int*)d_in[1], (const uint16_t*)d_in[2],
                                    (const uint16_t*)d_in[3], (char*)d_ws, (uint16_t*)d_out);
}
```

```cpp
#include <hip/hip_runtime.h>
#include <hip/hip_bf16.h>
#include <stdint.h>

typedef __attribute__((ext_vector_type(8))) short bf16x8;
typedef __attribute__((ext_vector_type(4))) float f32x4;
typedef __attribute__((ext_vector_type(4))) int i32x4;
typedef __attribute__((ext_vector_type(4))) unsigned u32x4;
typedef __attribute__((ext_vector_type(2))) unsigned u32x2;
typedef __attribute__((ext_vector_type(2))) float f32x2;
typedef __attribute__((ext_vector_type(2))) __bf16 bf16x2_t;
typedef __attribute__((address_space(1))) unsigned gu32;

constexpr int M_ = 2048, K_ = 1024, N_ = 3584;
constexpr int BM = 256, BN = 112, BK = 64, NT = K_ / BK;
constexpr int XB = BM * BK * 2;
constexpr int WB = BN * BK * 2;
constexpr int KCB = BN * 16;
constexpr int SB = XB + WB;
constexpr int NS = 3;
constexpr int HDR = 4096;
constexpr int LUT_OFF = -HDR;
constexpr int DUM_OFF = -HDR + 2048;
constexpr int FLG = 2560;
constexpr int STG_OFF = NS * SB;
constexpr int SHM_BYTES = HDR + NS * SB + WB;
constexpr size_t WIMG_BYTES = (size_t)32 * NT * WB;
constexpr size_t SYNC_OFF = (size_t)8 << 20;
constexpr unsigned SPIN_MAX = 1u << 17;
constexpr int POLLB_T = 5;
constexpr unsigned CODE_TOUCH_MAX = 10752;
constexpr int AUX_NT = 2, AUX_SC1 = 16;

#define WAIT_V(n) asm volatile("s_waitcnt vmcnt(%0)" ::"n"(n) : "memory")
#define WAIT_L(n) asm volatile("s_waitcnt lgkmcnt(%0)" ::"n"(n) : "memory")
#define BAR() do { asm volatile("" ::: "memory"); __builtin_amdgcn_s_barrier(); asm volatile("" ::: "memory"); } while (0)
#define SCHED() __builtin_amdgcn_sched_barrier(0)
#define SGB(m_, n_) __builtin_amdgcn_sched_group_barrier(m_, n_, 0)
#define LGKM0() do { SCHED(); __builtin_amdgcn_s_waitcnt(0xC07F); SCHED(); } while (0)
#define TAG_OF(e) ((e) * 0x9E3779B1u + 0x7F4A7C15u)
#define RLX_AGENT __ATOMIC_RELAXED, __HIP_MEMORY_SCOPE_AGENT

__device__ __forceinline__ unsigned pack_bf16(float a, float b) {
  f32x2 v = {a, b};
  bf16x2_t r = __builtin_convertvector(v, bf16x2_t);
  return __builtin_bit_cast(unsigned, r);
}
__device__ __forceinline__ int kstep_of(int s, int tm, int tn) {
  const int u = (int)(0xFEDCBA9765432180ull >> (4 * s)) & 15;
  return (u & 8) + ((tm + u + tn) & 7);
}

struct QRegs { i32x4 r[8]; };
struct LV { f32x2 v[4][4]; float sc[4]; };
__device__ __forceinline__ f32x2 pair_lookup(const char* lut, int q0, int q1) { return *(const f32x2*)(lut + (q0 << 3) + (q1 << 7)); }
__device__ __forceinline__ u32x4 q_pack(const LV& l, int i) {
  u32x4 o;
  o.x = pack_bf16(l.v[i][0].x * l.sc[i], l.v[i][0].y * l.sc[i]); o.y = pack_bf16(l.v[i][1].x * l.sc[i], l.v[i][1].y * l.sc[i]);
  o.z = pack_bf16(l.v[i][2].x * l.sc[i], l.v[i][2].y * l.sc[i]); o.w = pack_bf16(l.v[i][3].x * l.sc[i], l.v[i][3].y * l.sc[i]);
  return o;
}

__global__ __launch_bounds__(512, 2) void nf4_gemm(const uint16_t* __restrict__ x, const int* __restrict__ qw,
                                                   const uint16_t* __restrict__ cb, const uint16_t* __restrict__ am,
                                                   char* __restrict__ ws, uint16_t* __restrict__ out) {
  __shared__ __attribute__((aligned(1024))) char shm_raw[SHM_BYTES];
  char* const shm = shm_raw + HDR;
  unsigned long long pc0; asm volatile("s_getpc_b64 %0" : "=s"(pc0));
  unsigned ct;
  asm volatile("global_load_dword %0, %1, %2" : "=&v"(ct) : "v"(min((unsigned)(threadIdx.x * 128u), CODE_TOUCH_MAX)), "s"(pc0) : "memory");
  const int bid = blockIdx.x;
  const int xcd = bid & 7, bi = bid >> 3;
  const int tn = (xcd >> 2) * 16 + (bi & 15), tm = (xcd & 3) * 2 + (bi >> 4);
  const int rot = (tm + tn) & 7;
  const int tid = threadIdx.x, lane = tid & 63;
  const int wid = __builtin_amdgcn_readfirstlane(tid >> 6);
  const bool qact = (lane & 31) < 28;
  const int nc = qact ? (lane & 31) : 27;
  const int kc0 = wid < 4 ? 2 * wid : 4 * (wid & 1);
  const unsigned qvoff = (unsigned)(((kc0 + (lane >> 5)) * 8 * N_ + nc * 4) * 4);
  const unsigned avoff = (unsigned)(nc * 8);
  const unsigned qs0 = (unsigned)(tn * BN * 4);
  { unsigned t0_ = qvoff, t1_ = avoff; asm volatile("" : "+v"(t0_), "+v"(t1_)); }
  asm volatile("" :: "s"(qs0 + (unsigned)rot * (BK * N_ * 4)));
  SCHED();
  asm volatile("" :: "s"(x), "s"(qw), "s"(cb), "s"(am), "s"(ws), "s"(out));
  gu32* flags = (gu32*)(ws + SYNC_OFF + (size_t)tn * 512);
  char* lut = shm + LUT_OFF;
  const __amdgpu_buffer_rsrc_t rsW = __builtin_amdgcn_make_buffer_rsrc((void*)ws, (short)0, (int)WIMG_BYTES, 0x00020000);
  const __amdgpu_buffer_rsrc_t rsQ = __builtin_amdgcn_make_buffer_rsrc((void*)qw, (short)0, K_ * N_ * 4, 0x00020000);
  const uint16_t* am0 = am + tn * BN;
  const unsigned zero = 0;
#define Q_ROW(Q, j, so_) asm volatile("buffer_load_dwordx4 %0, %1, %2, %3 offen nt" : "=&v"(Q.r[j]) : "v"(qvoff), "s"(rsQ), "s"((so_) + (j) * (N_ * 4)) : "memory")
#define Q_ROWS(Q, so_) do { _Pragma("unroll") for (int j_ = 0; j_ < 8; j_++) Q_ROW(Q, j_, so_); } while (0)
#define Q_SC(S, ks) asm volatile("global_load_dwordx2 %0, %1, %2" : "=&v"(S) : "v"(avoff), "s"(am0 + (ks) * N_) : "memory")
#define Q_SO(ks) (qs0 + (unsigned)(ks) * (BK * N_ * 4))
#define TAG_LOAD(T) asm volatile("global_load_dword %0, %1, %2 offset:256 sc1" : "=&v"(T) : "v"(zero), "s"(flags) : "memory")
#define Q_PIN(q) asm volatile("" : "+v"(q.r[0]), "+v"(q.r[1]), "+v"(q.r[2]), "+v"(q.r[3]), "+v"(q.r[4]), "+v"(q.r[5]), "+v"(q.r[6]), "+v"(q.r[7]))
#define SCALES(S) do { asm volatile("" : "+v"(S));                                                                                \
      lv.sc[0] = __builtin_bit_cast(float, S.x << 16); lv.sc[1] = __builtin_bit_cast(float, S.x & 0xffff0000u);                   \
      lv.sc[2] = __builtin_bit_cast(float, S.y << 16); lv.sc[3] = __builtin_bit_cast(float, S.y & 0xffff0000u); } while (0)
#define ROUND(Q, dst) do { Q_PIN(Q);                                                                                              \
      _Pragma("unroll") for (int i_ = 0; i_ < 4; i_++) _Pragma("unroll") for (int a_ = 0; a_ < 4; a_++)                           \
        lv.v[i_][a_] = pair_lookup(lut, Q.r[2 * a_][i_], Q.r[2 * a_ + 1][i_]);                                                    \
      SCHED();                                                                                                                    \
      char* d_ = shm + (qact ? (dst) + io : DUM_OFF);                                                                             \
      _Pragma("unroll") for (int i_ = 0; i_ < 4; i_++) *(u32x4*)(d_ + ((i_ << 4) ^ isw)) = q_pack(lv, i_);                        \
      SCHED(); } while (0)
#define PUBLISH(src, ks, kcp) do { WAIT_L(0); if (lane < 56) {                                                                    \
        const unsigned g_ = (unsigned)((tn * NT + (ks)) * WB + (kcp) * (2 * KCB) + lane * 16); u32x4 pv_[4];                      \
        _Pragma("unroll") for (int i_ = 0; i_ < 4; i_++) pv_[i_] = *(const u32x4*)(shm + (src) + lane * 16 + i_ * 896);           \
        _Pragma("unroll") for (int i_ = 0; i_ < 4; i_++) __builtin_amdgcn_raw_buffer_store_b128(pv_[i_], rsW, g_ + i_ * 896, 0, AUX_SC1); } \
      SCHED(); } while (0)
  LV lv;
  const __amdgpu_buffer_rsrc_t rsO = __builtin_amdgcn_make_buffer_rsrc((void*)out, (short)0, M_ * N_ * 2, 0x00020000);
  const int f0 = (tn & 3) * 2;
  unsigned vo; int ro;
#define O_ADDR(whole) do { const int r8 = lane >> 3, cq = lane & 7;                                                               \
      vo = (unsigned)(((tm * BM + (wid & 3) * 64 + r8) * N_ + tn * BN) * 2 + ((whole) ? f0 + cq : cq < f0 ? cq : cq + 8) * 16);    \
      if (!(whole) && cq >= 6) vo = 0x7fffffffu;                                                                                  \
      ro = HDR + ((whole) ? SB : 0) + (wid & 3) * 8192 + r8 * 128 + ((cq ^ r8) << 4);                                             \
      asm volatile("" : "+v"(vo), "+v"(ro)); } while (0)
#define O_RD(i_) (*(const u32x4*)(shm_raw + ro + (i_) * 1024))
#define O_ST(v_, i_, aux) __builtin_amdgcn_raw_buffer_store_b128(v_, rsO, vo + (i_) * 8 * (N_ * 2), 0, aux)
#define P0_OF() ({ const int c_ = nc >> 1, v_ = c_ < f0 ? c_ + 8 : c_ < f0 + 8 ? c_ - f0 : c_, n_ = 2 * v_ + (nc & 1);              \
      n_ < 24 ? 32 * (n_ >> 3) + 16 * (n_ & 1) + 4 * ((n_ & 7) >> 1) : n_ * 4; })

  if (__builtin_expect(wid < 4, 1)) {
    QRegs q; u32x2 qsv; unsigned c0, c1, tagv;
    asm volatile("global_load_ushort %0, %2, %4\n\tglobal_load_ushort %1, %3, %4" : "=&v"(c0), "=&v"(c1)
                 : "v"((unsigned)((lane & 15) * 2)), "v"((unsigned)((wid * 4 + (lane >> 4)) * 2)), "s"(cb) : "memory");
    Q_ROWS(q, Q_SO(rot));
    Q_SC(qsv, rot);
    TAG_LOAD(tagv);
    SCHED();
    BAR();
    const int p0 = P0_OF();
    const int io = (lane >> 5) * KCB + p0 * 16, isw = ((p0 >> 3) & 3) << 4;
    f32x4 acc[4][7];
#pragma unroll
    for (int m = 0; m < 4; m++)
#pragma unroll
      for (int n = 0; n < 7; n++) { acc[m][n] = (f32x4){0.f, 0.f, 0.f, 0.f}; asm volatile("" : "+v"(acc[m][n])); }
    WAIT_V(10);
    asm volatile("" : "+v"(c0), "+v"(c1)); asm volatile("" :: "v"(ct));
    *(u32x2*)(lut + tid * 8) = (u32x2){c0 << 16, c1 << 16};
    int fa = FLG + wid * 320 + lane * 4;
    asm volatile("" : "+v"(fa));
    *(unsigned*)(shm_raw + fa + 32) = 0u;
    WAIT_L(0);
    BAR();
    WAIT_V(0);
    SCALES(qsv);
    ROUND(q, XB + kc0 * KCB);
    PUBLISH(XB + kc0 * KCB, rot, wid);
    BAR();
    const int fr = lane & 15, fq = lane >> 4;
    const int xo0 = (wid * 64 + fr) * 128 + ((fq ^ (fr >> 1)) << 4), xo1 = xo0 ^ 64;
    const int woe = XB + fq * KCB + ((fr ^ (fr >> 3)) << 4), woo = woe ^ 32;
    bf16x8 xa[4], wa[7], xb[4], wb[7];
#define RD(xf, wf, base, wbase_, xo, hf) do {                                                         \
      _Pragma("unroll") for (int n_ = 0; n_ < 7; n_++) wf[n_] = *(const bf16x8*)((wbase_) + ((n_ & 1) ? woo : woe) + (hf) * (4 * KCB) + n_ * 256); \
      _Pragma("unroll") for (int m_ = 0; m_ < 4; m_++) xf[m_] = *(const bf16x8*)((base) + (xo) + m_ * 2048); } while (0)
#define MMR(xf, wf, i0, i1) do { _Pragma("unroll") for (int i_ = (i0); i_ < (i1); i_++)               \
      acc[i_ / 7][i_ % 7] = __builtin_amdgcn_mfma_f32_16x16x32_bf16(wf[i_ % 7], xf[i_ / 7], acc[i_ / 7][i_ % 7], 0, 0, 0); } while (0)
    O_ADDR(false);
    int e0 = HDR + wid * 8192 + fr * 128 + ((fq ^ (fr & 7)) << 4), e1 = e0 ^ 64;
    int e2 = HDR + wid * 8192 + fr * 128 + (((4 + (fq >> 1)) ^ (fr & 7)) << 4) + (fq & 1) * 8;
    asm volatile("" : "+v"(e0), "+v"(e1), "+v"(e2));
    WAIT_V(0);
    asm volatile("" : "+v"(tagv));
    if (lane == 0) __hip_atomic_store(flags + rot * 4 + wid, TAG_OF(tagv), RLX_AGENT);
    BAR();
#pragma unroll 1
    for (int ks = 0; ks < 2; ks++) {
      RD(xa, wa, shm, shm + ks * (4 * KCB), xo0 ^ (ks * 64), 0);
      MMR(xa, wa, 0, 28);
      SCHED();
    }
    asm volatile(".rept %0\n\ts_nop 0\n\t.endr" :: "n"(1));
    BAR();
    RD(xa, wa, shm + SB, shm + (STG_OFF - XB), xo0, 0);
    int ae = woe + (STG_OFF - XB), ao = woo + (STG_OFF - XB), a0 = xo0 + SB, a1 = xo1 + SB, so = SB;
#define RDA(xf, wf, ax, hf) do {                                                                      \
      _Pragma("unroll") for (int n_ = 0; n_ < 7; n_++) wf[n_] = *(const bf16x8*)(shm + ((n_ & 1) ? ao : ae) + (hf) * (4 * KCB) + n_ * 256); \
      _Pragma("unroll") for (int m_ = 0; m_ < 4; m_++) xf[m_] = *(const bf16x8*)(shm + (ax) + m_ * 2048); } while (0)
#define INTER11() do { _Pragma("unroll") for (int i_ = 0; i_ < 11; i_++) { SGB(0x8, 2); SGB(0x100, 1); } } while (0)
    asm volatile("" : "+v"(ae), "+v"(ao), "+v"(a1));
#pragma unroll 1
    for (int t = 1; t < NT - 2; t++) {
      SCHED();
      RDA(xb, wb, a1, 1);
      MMR(xa, wa, 0, 27);
      so = so == 2 * SB ? 0 : so + SB;
      ae = woe + so; ao = woo + so; a0 = xo0 + so; a1 = xo1 + so;
      INTER11(); SGB(0x8, 1); SGB(0x4, 3); SGB(0x8, 1); SGB(0x2, 2); SGB(0x8, 1); SGB(0x2, 2); SGB(0x8, 2);
      asm volatile("" : "+v"(ae), "+v"(ao), "+v"(a0), "+v"(a1));
      LGKM0();
      MMR(xa, wa, 27, 28);
      SCHED();
      BAR();
      SCHED();
      RDA(xa, wa, a0, 0);
      MMR(xb, wb, 0, 28);
      INTER11(); SGB(0x8, 4); SGB(0x4, 2); SGB(0x8, 2);
    }
    SCHED();
#define LD(o_) (*(const bf16x8*)(shm + (o_)))
#define MF(m_, n_, xv, wf) do { asm volatile("v_mfma_f32_16x16x32_bf16 %0, %1, %2, %0" : "+v"(acc[m_][n_]) : "v"(wf[n_]), "v"(xv)); SCHED(); } while (0)
#define RX(xf, b_, m_) xf[m_] = LD((b_) + (m_) * 2048)
#define RW(wf, b_, hf, n_) wf[n_] = LD((b_) + (hf) * (4 * KCB) + (n_) * 256)
#define CV(m_, j_) pk[m_][j_] = pack_bf16(acc[m_][(j_) / 2][((j_) & 1) * 2], acc[m_][(j_) / 2][((j_) & 1) * 2 + 1])
#define WL(n_) do { SCHED(); __builtin_amdgcn_s_waitcnt(0xC07F | ((n_) << 8)); SCHED(); } while (0)
#define W64(m_, n_) *(u32x2*)(shm_raw + ((n_) == 6 ? e2 : ((n_) & 2 ? e1 : e0) + ((n_) & 1) * 8) + ((n_) < 4 ? SB : 0) + (m_) * 2048) = (u32x2){pk[m_][2 * (n_)], pk[m_][2 * (n_) + 1]}
#define FLAG(u_) do { asm volatile("" ::: "memory"); *(int*)(shm_raw + fa + 4 * (u_)) = fa; asm volatile("" ::: "memory"); } while (0)
    {
      bf16x8 xc[4], xd[4], wc[7], wd[7];
      unsigned pk[4][14];
      u32x4 ov[8];
      MF(0, 0, xa[0], wa); RX(xb, a1, 0); SCHED();
      MF(0, 1, xa[0], wa); RW(wb, ae, 1, 0); SCHED();
      MF(0, 2, xa[0], wa); RW(wb, ao, 1, 1); SCHED();
      MF(0, 3, xa[0], wa); RW(wb, ae, 1, 2); SCHED();
      MF(1, 0, xa[1], wa); RW(wb, ao, 1, 3); SCHED();
      MF(1, 1, xa[1], wa); RX(xb, a1, 1); SCHED();
      MF(1, 2, xa[1], wa);
      BAR();
      SCHED();
      MF(1, 3, xa[1], wa); RX(xc, xo0, 0); SCHED();
      MF(2, 0, xa[2], wa); RW(wc, woe, 0, 0); SCHED();
      MF(2, 1, xa[2], wa); RX(xd, xo1, 0); SCHED();
      MF(2, 2, xa[2], wa); RW(wd, woe, 1, 0); SCHED();
      MF(2, 3, xa[2], wa); RW(wc, woo, 0, 1); SCHED();
      MF(3, 0, xa[3], wa); RW(wd, woo, 1, 1); SCHED();
      MF(3, 1, xa[3], wa); RW(wc, woe, 0, 2); SCHED();
      MF(3, 2, xa[3], wa); RW(wd, woe, 1, 2); SCHED();
      MF(3, 3, xa[3], wa); RW(wc, woo, 0, 3); WL(5); SCHED();
      MF(0, 0, xb[0], wb); RW(wd, woo, 1, 3); SCHED();
      MF(0, 0, xc[0], wc); RX(xc, xo0, 1); SCHED();
      MF(0, 0, xd[0], wd); RX(xd, xo1, 1); SCHED();
      MF(0, 1, xb[0], wb); WL(4); SCHED();
      MF(0, 1, xc[0], wc);
      MF(0, 1, xd[0], wd); CV(0, 0); CV(0, 1); SCHED();
      MF(0, 2, xb[0], wb); W64(0, 0); SCHED();
      MF(0, 2, xc[0], wc); RX(xb, a1, 2); WL(2); SCHED();
      MF(0, 2, xd[0], wd); CV(0, 2); CV(0, 3); SCHED();
      MF(0, 3, xb[0], wb); W64(0, 1); SCHED();
      MF(0, 3, xc[0], wc); RX(xc, xo0, 2); RX(xd, xo1, 2); SCHED();
      MF(0, 3, xd[0], wd); CV(0, 4); CV(0, 5); SCHED();
      MF(1, 0, xb[1], wb); W64(0, 2); SCHED();
      MF(1, 0, xc[1], wc); RX(xb, a1, 3); RX(xc, xo0, 3); SCHED();
      MF(1, 0, xd[1], wd); CV(0, 6); CV(0, 7); SCHED();
      MF(1, 1, xb[1], wb); W64(0, 3); SCHED();
      MF(1, 1, xc[1], wc); FLAG(1); SCHED();
      MF(1, 1, xd[1], wd); CV(1, 0); CV(1, 1); SCHED();
      MF(1, 2, xb[1], wb); W64(1, 0); SCHED();
      MF(1, 2, xc[1], wc); RX(xd, xo1, 3); SCHED();
      MF(1, 2, xd[1], wd); CV(1, 2); CV(1, 3); SCHED();
      MF(1, 3, xb[1], wb); W64(1, 1); SCHED();
      MF(1, 3, xc[1], wc); RW(wb, ae, 1, 4); WL(6); SCHED();
      MF(1, 3, xd[1], wd); CV(1, 4); CV(1, 5); SCHED();
      MF(2, 0, xb[2], wb); W64(1, 2); SCHED();
      MF(2, 0, xc[2], wc); RW(wc, woe, 0, 4); RW(wd, woe, 1, 4); SCHED();
      MF(2, 0, xd[2], wd); CV(1, 6); CV(1, 7); SCHED();
      MF(2, 1, xb[2], wb); W64(1, 3); SCHED();
      MF(2, 1, xc[2], wc); FLAG(2); SCHED();
      MF(2, 1, xd[2], wd); CV(2, 0); CV(2, 1); SCHED();
      MF(2, 2, xb[2], wb); W64(2, 0); SCHED();
      MF(2, 2, xc[2], wc); RW(wb, ao, 1, 5); WL(4); SCHED();
      MF(2, 2, xd[2], wd); CV(2, 2); CV(2, 3); SCHED();
      MF(2, 3, xb[2], wb); W64(2, 1); SCHED();
      MF(2, 3, xc[2], wc); RW(wc, woo, 0, 5); RW(wd, woo, 1, 5); SCHED();
      MF(2, 3, xd[2], wd); CV(2, 4); CV(2, 5); SCHED();
      MF(3, 0, xb[3], wb); W64(2, 2); SCHED();
      MF(3, 0, xc[3], wc); RW(wb, ae, 1, 6); RW(wc, woe, 0, 6); SCHED();
      MF(3, 0, xd[3], wd); CV(2, 6); CV(2, 7); SCHED();
      MF(3, 1, xb[3], wb); W64(2, 3); SCHED();
      MF(3, 1, xc[3], wc); FLAG(3); SCHED();
      MF(3, 1, xd[3], wd); CV(3, 0); CV(3, 1); SCHED();
      MF(3, 2, xb[3], wb); W64(3, 0); SCHED();
      MF(3, 2, xc[3], wc); RW(wd, woe, 1, 6); SCHED();
      MF(3, 2, xd[3], wd); CV(3, 2); CV(3, 3); SCHED();
      MF(3, 3, xb[3], wb); W64(3, 1); SCHED();
      MF(3, 3, xc[3], wc); WL(5); SCHED();
      MF(3, 3, xd[3], wd); CV(3, 4); CV(3, 5); SCHED();
      MF(0, 4, xa[0], wa); W64(3, 2); SCHED();
      MF(0, 4, xb[0], wb);
      MF(0, 4, xc[0], wc); CV(3, 6); CV(3, 7); SCHED();
      MF(0, 4, xd[0], wd); W64(3, 3); SCHED();
      MF(0, 5, xa[0], wa); FLAG(4); SCHED();
      MF(0, 5, xb[0], wb);
      MF(0, 5, xc[0], wc); CV(0, 8); CV(0, 9); SCHED();
      MF(0, 5, xd[0], wd); W64(0, 4); SCHED();
      MF(0, 6, xa[0], wa); WL(5); SCHED();
      MF(0, 6, xb[0], wb);
      MF(0, 6, xc[0], wc); CV(0, 10); CV(0, 11); SCHED();
      MF(0, 6, xd[0], wd); W64(0, 5); SCHED();
      MF(1, 4, xa[1], wa);
      MF(1, 4, xb[1], wb);
      MF(1, 4, xc[1], wc); CV(0, 12); CV(0, 13); SCHED();
      MF(1, 4, xd[1], wd); W64(0, 6); SCHED();
      MF(1, 5, xa[1], wa); FLAG(5); SCHED();
      MF(1, 5, xb[1], wb);
      MF(1, 5, xc[1], wc); CV(1, 8); CV(1, 9); SCHED();
      MF(1, 5, xd[1], wd); W64(1, 4); SCHED();
      MF(1, 6, xa[1], wa);
      MF(1, 6, xb[1], wb);
      MF(1, 6, xc[1], wc); CV(1, 10); CV(1, 11); SCHED();
      MF(1, 6, xd[1], wd); W64(1, 5); SCHED();
      MF(2, 4, xa[2], wa);
      MF(2, 4, xb[2], wb);
      MF(2, 4, xc[2], wc); CV(1, 12); CV(1, 13); SCHED();
      MF(2, 4, xd[2], wd); W64(1, 6); SCHED();
      MF(2, 5, xa[2], wa); FLAG(6); SCHED();
      MF(2, 5, xb[2], wb);
      MF(2, 5, xc[2], wc); CV(2, 8); CV(2, 9); SCHED();
      MF(2, 5, xd[2], wd); W64(2, 4); SCHED();
      MF(2, 6, xa[2], wa);
      MF(2, 6, xb[2], wb);
      MF(2, 6, xc[2], wc); CV(2, 10); CV(2, 11); SCHED();
      MF(2, 6, xd[2], wd); W64(2, 5); SCHED();
      MF(3, 4, xa[3], wa);
      MF(3, 4, xb[3], wb);
      MF(3, 4, xc[3], wc); CV(2, 12); CV(2, 13); SCHED();
      MF(3, 4, xd[3], wd); W64(2, 6); SCHED();
      MF(3, 5, xa[3], wa); ov[4] = O_RD(4); ov[5] = O_RD(5); SCHED();
      MF(3, 5, xb[3], wb);
      MF(3, 5, xc[3], wc); CV(3, 8); CV(3, 9); SCHED();
      MF(3, 5, xd[3], wd); W64(3, 4); SCHED();
      MF(3, 6, xa[3], wa);
      MF(3, 6, xb[3], wb);
      MF(3, 6, xc[3], wc); CV(3, 10); CV(3, 11); SCHED();
      MF(3, 6, xd[3], wd); W64(3, 5); SCHED();
      asm volatile("s_nop 7");
      SCHED();
      CV(3, 12); CV(3, 13); W64(3, 6); SCHED();
      ov[6] = O_RD(6); ov[7] = O_RD(7); SCHED();
      O_ST(ov[4], 4, 0); O_ST(ov[5], 5, 0);
      O_ST(ov[6], 6, 0); O_ST(ov[7], 7, 0);
    }
  } else {
  if (wid < 6) {
    const int h = wid - 4;
    unsigned xv[16];
#pragma unroll
    for (int i = 0; i < 16; i++) xv[i] = (unsigned)((8 * i + (lane >> 3)) * (K_ * 2) + (((lane & 7) ^ ((i & 1) * 4 + (lane >> 4))) << 4));
    const char* xbase = (const char*)(x + (size_t)(tm * BM + h * 128) * K_);
    const unsigned ldsx = (unsigned)(size_t)(shm) + h * 16384;
#define X8(o, l_, b_)                                                                                                             \
      asm volatile("s_mov_b32 m0, %8\n\ts_nop 0\n\tglobal_load_lds_dwordx4 %0, %9\n\t"                                            \
                   "s_add_u32 m0, m0, 0x400\n\ts_nop 0\n\tglobal_load_lds_dwordx4 %1, %9\n\t"                                     \
                   "s_add_u32 m0, m0, 0x400\n\ts_nop 0\n\tglobal_load_lds_dwordx4 %2, %9\n\t"                                     \
                   "s_add_u32 m0, m0, 0x400\n\ts_nop 0\n\tglobal_load_lds_dwordx4 %3, %9\n\t"                                     \
                   "s_add_u32 m0, m0, 0x400\n\ts_nop 0\n\tglobal_load_lds_dwordx4 %4, %9\n\t"                                     \
                   "s_add_u32 m0, m0, 0x400\n\ts_nop 0\n\tglobal_load_lds_dwordx4 %5, %9\n\t"                                     \
                   "s_add_u32 m0, m0, 0x400\n\ts_nop 0\n\tglobal_load_lds_dwordx4 %6, %9\n\t"                                     \
                   "s_add_u32 m0, m0, 0x400\n\ts_nop 0\n\tglobal_load_lds_dwordx4 %7, %9"                                         \
                   :: "v"(xv[o]), "v"(xv[o + 1]), "v"(xv[o + 2]), "v"(xv[o + 3]), "v"(xv[o + 4]), "v"(xv[o + 5]), "v"(xv[o + 6]), "v"(xv[o + 7]), \
                      "s"(l_), "s"(b_) : "memory", "scc")
#define ISSUE_X(s, slot) do { const char* b_ = xbase + kstep_of(s, tm, tn) * 128; const unsigned l_ = ldsx + (slot) * SB;       \
      X8(0, l_, b_); X8(8, l_ + 8192, b_); } while (0)
    BAR();
    BAR();
    BAR();
    ISSUE_X(0, 0);
    ISSUE_X(1, 1);
    WAIT_V(16);
    asm volatile("" :: "v"(ct));
    BAR();
    int xslot = 2;
#pragma unroll 1
    for (int t = -1; t < NT - 2; t++) {
      if (t + 3 < NT) { ISSUE_X(t + 3, xslot); xslot = xslot == NS - 1 ? 0 : xslot + 1; WAIT_V(16); } else { WAIT_V(0); }
      BAR();
    }
  } else {
    QRegs qa, qb; u32x2 sb; unsigned tagv;
    constexpr unsigned RO = 16 * N_ * 4;
    Q_SC(sb, rot + 8); TAG_LOAD(tagv);
    const int p0 = P0_OF();
    const int io = (lane >> 5) * KCB + p0 * 16, isw = ((p0 >> 3) & 3) << 4;
    BAR();
    Q_ROWS(qa, Q_SO(rot + 8));
    Q_ROWS(qb, Q_SO(rot + 8) + RO);
    BAR();
    BAR();
    WAIT_V(8);
    asm volatile("" :: "v"(ct));
    SCALES(sb);
    ROUND(qa, STG_OFF + kc0 * KCB);
    WAIT_V(0);
    ROUND(qb, STG_OFF + (kc0 + 2) * KCB);
    WAIT_L(0);
    asm volatile("" : "+v"(tagv));
    const unsigned tag = TAG_OF(tagv);
   if (wid == 6) {
    const char* wbase = ws + (size_t)tn * NT * WB;
    const unsigned wv = (unsigned)(lane * 16), wv2 = wv + 4096, wv3 = wv + 8192, wv4 = wv + 12288;
    const unsigned ldsw = (unsigned)(size_t)(shm) + XB;
#define ISSUE_W(s, slot) do { const char* b_ = wbase + (size_t)kstep_of(s, tm, tn) * WB; const unsigned l_ = ldsw + (slot) * SB; \
      asm volatile("s_mov_b32 m0, %4\n\ts_nop 0\n\t"                                                                              \
                   "global_load_lds_dwordx4 %0, %5 sc1\n\tglobal_load_lds_dwordx4 %0, %5 offset:1024 sc1\n\t"                     \
                   "global_load_lds_dwordx4 %0, %5 offset:2048 sc1\n\tglobal_load_lds_dwordx4 %0, %5 offset:3072 sc1\n\t"         \
                   "s_add_u32 m0, m0, 0x1000\n\ts_nop 0\n\t"                                                                      \
                   "global_load_lds_dwordx4 %1, %5 sc1\n\tglobal_load_lds_dwordx4 %1, %5 offset:1024 sc1\n\t"                     \
                   "global_load_lds_dwordx4 %1, %5 offset:2048 sc1\n\tglobal_load_lds_dwordx4 %1, %5 offset:3072 sc1\n\t"         \
                   "s_add_u32 m0, m0, 0x1000\n\ts_nop 0\n\t"                                                                      \
                   "global_load_lds_dwordx4 %2, %5 sc1\n\tglobal_load_lds_dwordx4 %2, %5 offset:1024 sc1\n\t"                     \
                   "global_load_lds_dwordx4 %2, %5 offset:2048 sc1\n\tglobal_load_lds_dwordx4 %2, %5 offset:3072 sc1\n\t"         \
                   "s_add_u32 m0, m0, 0x1000\n\ts_nop 0\n\t"                                                                      \
                   "global_load_lds_dwordx4 %3, %5 sc1\n\tglobal_load_lds_dwordx4 %3, %5 offset:1024 sc1"                         \
                   :: "v"(wv), "v"(wv2), "v"(wv3), "v"(wv4), "s"(l_), "s"(b_) : "memory", "scc"); } while (0)
    const unsigned fvo = (unsigned)(lane * 4);
#define SET_RDY(f_) do { const unsigned long long b_ = __ballot((f_) == tag);                                                     \
      rda = (unsigned)__builtin_amdgcn_readfirstlane((unsigned)b_); rdb = (unsigned)__builtin_amdgcn_readfirstlane((unsigned)(b_ >> 32)); } while (0)
#define POLL_BLOCK() do { unsigned f_; asm volatile("global_load_dword %0, %1, %2 sc1\n\ts_waitcnt vmcnt(0)" : "=&v"(f_) : "v"(fvo), "s"(flags) : "memory"); \
      SET_RDY(f_); } while (0)
#define READY(s) (((((s) > 8 ? rdb : rda) >> ((kstep_of(s, tm, tn) & 7) * 4)) & 15u) == 15u)
#define NEXT_W() do { nw++; wslot = wslot == NS - 1 ? 0 : wslot + 1; } while (0)
    unsigned rda = 0, rdb = 0, spins = 0;
    BAR();
    int nw = 2, wslot = 2;
    unsigned fv = tagv;
    bool ppoll = false, preg = false;
#pragma unroll 1
    for (int t = -1; t < NT - 2; t++) {
      const int lim = t + 3 < NT ? t + 3 : NT - 1;
      if (preg) { WAIT_V(14); } else { WAIT_V(0); }
      asm volatile("" : "+v"(fv));
      if (ppoll) SET_RDY(fv);
      if (nw <= t + 2) {
#pragma unroll 1
        while (!READY(nw)) { if (++spins > SPIN_MAX) { rda = ~0u; rdb = ~0u; break; } POLL_BLOCK(); }
        ISSUE_W(nw, wslot); NEXT_W();
      }
      ppoll = spins <= SPIN_MAX && (t < POLLB_T ? rda != ~0u : rdb != ~0u);
      asm volatile("global_load_dword %0, %1, %2 sc1" : "+v"(fv) : "v"(fvo), "s"(ppoll ? (const char*)flags : (const char*)am) : "memory");
      preg = nw <= lim && READY(nw);
      if (preg) { ISSUE_W(nw, wslot); NEXT_W(); WAIT_V(15); } else { WAIT_V(1); }
      BAR();
    }
    WAIT_V(0);
    asm volatile("" : "+v"(fv));
    if (tm == 0) {
#pragma unroll 1
      while ((rda & rdb) != ~0u && ++spins < SPIN_MAX) POLL_BLOCK();
      if (lane == 0) __hip_atomic_store(flags + 64, tag, RLX_AGENT);
    }
   } else {
    BAR();
    BAR();
#pragma unroll 1
    for (int i = 0; i < 2; i++) {
      PUBLISH(STG_OFF + 4 * i * KCB, rot + 8, 2 * i);
      PUBLISH(STG_OFF + (4 * i + 2) * KCB, rot + 8, 2 * i + 1);
      BAR();
    }
    BAR();
    WAIT_V(0);
    if (lane < 4) __hip_atomic_store(flags + (rot + 8) * 4 + lane, tag, RLX_AGENT);
#pragma unroll 1
    for (int t = 4; t < NT - 1; t++) BAR();
   }
  }
    unsigned fp = (unsigned)(size_t)shm_raw + FLG + (wid & 3) * 320 + 256;
#define UNIT(aux) do { unsigned f_; u32x4 o0_, o1_;                                                                               \
      do { asm volatile("ds_read_b32 %0, %3\n\tds_read_b128 %1, %4\n\tds_read_b128 %2, %4 offset:1024\n\ts_waitcnt lgkmcnt(0)"     \
                        : "=&v"(f_), "=&v"(o0_), "=&v"(o1_) : "v"(fp), "v"(ro) : "memory");                                       \
      } while (__builtin_amdgcn_readfirstlane(f_) == 0);                                                                          \
      O_ST(o0_, 0, aux); O_ST(o1_, 1, aux); vo += 16 * (N_ * 2); ro += 2048; fp += 4; } while (0)
    O_ADDR(false);
    const unsigned vb = vo; const int rb = ro;
    O_ADDR(true);
    { unsigned f_; do { asm volatile("ds_read_b32 %0, %1\n\ts_waitcnt lgkmcnt(0)" : "=v"(f_) : "v"(fp) : "memory"); } while (__builtin_amdgcn_readfirstlane(f_) == 0); }
#pragma unroll 1
    for (int u = 0; u < 4; u++) UNIT(AUX_NT);
    vo = vb; ro = rb;
#pragma unroll 1
    for (int u = 4; u < 6; u++) UNIT(0);
  }
  asm volatile("" ::: "memory");
}

extern "C" void kernel_launch(void* const* d_in, const int* in_sizes, int n_in, void* d_out, int out_size, void* d_ws,
                              size_t ws_size, hipStream_t stream) {
  nf4_gemm<<<256, 512, 0, stream>>>((const uint16_t*)d_in[0], (const int*)d_in[1], (const uint16_t*)d_in[2],
                                    (const uint16_t*)d_in[3], (char*)d_ws, (uint16_t*)d_out);
}
```

```cpp
#include <hip/hip_runtime.h>
#include <hip/hip_bf16.h>
#include <stdint.h>

typedef __attribute__((ext_vector_type(8))) short bf16x8;
typedef __attribute__((ext_vector_type(4))) float f32x4;
typedef __attribute__((ext_vector_type(4))) int i32x4;
typedef __attribute__((ext_vector_type(4))) unsigned u32x4;
typedef __attribute__((ext_vector_type(2))) unsigned u32x2;
typedef __attribute__((ext_vector_type(2))) float f32x2;
typedef __attribute__((ext_vector_type(2))) __bf16 bf16x2_t;
typedef __attribute__((address_space(1))) unsigned gu32;

constexpr int M_ = 2048, K_ = 1024, N_ = 3584;
constexpr int BM = 256, BN = 112, BK = 64, NT = K_ / BK;
constexpr int XB = BM * BK * 2;
constexpr int WB = BN * BK * 2;
constexpr int KCB = BN * 16;
constexpr int SB = XB + WB;
constexpr int NS = 3;
constexpr int HDR = 4096;
constexpr int LUT_OFF = -HDR;
constexpr int FLG = 2560;
constexpr int CNT_OFF = -HDR + 2304;
constexpr int STG_OFF = NS * SB;
constexpr int SHM_BYTES = HDR + NS * SB + WB;
constexpr size_t WIMG_BYTES = (size_t)32 * NT * WB;
constexpr size_t SYNC_OFF = (size_t)8 << 20;
constexpr unsigned SPIN_MAX = 1u << 17;
constexpr unsigned CODE_TOUCH_MAX = 13800;
constexpr int AUX_NT = 2, AUX_SC1 = 16;

#define WAIT_V(n) asm volatile("s_waitcnt vmcnt(%0)" ::"n"(n) : "memory")
#define WAIT_L(n) asm volatile("s_waitcnt lgkmcnt(%0)" ::"n"(n) : "memory")
#define BAR() do { asm volatile("" ::: "memory"); __builtin_amdgcn_s_barrier(); asm volatile("" ::: "memory"); } while (0)
#define SCHED() __builtin_amdgcn_sched_barrier(0)
#define SGB(m_, n_) __builtin_amdgcn_sched_group_barrier(m_, n_, 0)
#define LGKM0() do { SCHED(); __builtin_amdgcn_s_waitcnt(0xC07F); SCHED(); } while (0)
#define TAG_OF(e) ((e) * 0x9E3779B1u + 0x7F4A7C15u)
#define RLX_AGENT __ATOMIC_RELAXED, __HIP_MEMORY_SCOPE_AGENT

__device__ __forceinline__ unsigned pack_bf16(float a, float b) {
  f32x2 v = {a, b};
  bf16x2_t r = __builtin_convertvector(v, bf16x2_t);
  return __builtin_bit_cast(unsigned, r);
}
__device__ __forceinline__ unsigned long long order_of(int tm, int tn) {
  const int t = tn & 15;
  const unsigned long long a = ((0x765432765432ull >> (8 * (t % 3))) & 0xffffff) ^ ((t & 1) * 0x111111);
  const unsigned long long b = (0xFEDCBA9FEDCBA9ull >> (4 * (t % 7))) & 0xfffffff;
  return (0x810 | a << 12 | b << 36) ^ (tm * 0x1111111111111111ull);
}

struct QRegs { i32x4 r[8]; };
__device__ __forceinline__ float smul(float a, float b) { float r; asm("v_mul_f32 %0, %1, %2" : "=v"(r) : "v"(a), "v"(b)); return r; }

__global__ __launch_bounds__(512, 2) void nf4_gemm(const uint16_t* __restrict__ x, const int* __restrict__ qw,
                                                   const uint16_t* __restrict__ cb, const uint16_t* __restrict__ am,
                                                   char* __restrict__ ws, uint16_t* __restrict__ out) {
  __shared__ __attribute__((aligned(1024))) char shm_raw[SHM_BYTES];
  char* const shm = shm_raw + HDR;
  unsigned long long pc0; asm volatile("s_getpc_b64 %0" : "=s"(pc0));
  unsigned ct;
  asm volatile("global_load_dword %0, %1, %2" : "=&v"(ct) : "v"(min((unsigned)((threadIdx.x & 15) * 1024 + ((blockIdx.x >> 3) & 7) * 128), CODE_TOUCH_MAX)), "s"(pc0) : "memory");
  const int bid = blockIdx.x;
  const int xcd = bid & 7, bi = bid >> 3;
  const int tn = (xcd >> 2) * 16 + (bi & 15), tm = (xcd & 3) * 2 + ((bi >> 4) & 1);
  const int k1 = tm ^ 1, k2 = tm + 8;
  const int tid = threadIdx.x, lane = tid & 63;
  const int wid = __builtin_amdgcn_readfirstlane(tid >> 6);
  const bool qact = (lane & 31) < 28;
  const int nc = qact ? (lane & 31) : 27;
  const int kc0 = 2 * (wid & 3);
  const unsigned qvoff = (unsigned)(((kc0 + (lane >> 5)) * 8 * N_ + nc * 4) * 4);
  const unsigned avoff = (unsigned)(nc * 8);
  const unsigned qs0 = (unsigned)(tn * BN * 4);
  const unsigned xtv = (unsigned)((lane & 3) * (K_ * 2) + (((0x810 >> (4 * min(lane >> 2, 2))) & 15) ^ tm) * 128 + (tm * BM + (tn & 15) * 16 + (wid & 3) * 4) * (K_ * 2));
  asm volatile("" :: "v"(qvoff), "v"(avoff), "v"(xtv));
  asm volatile("" :: "s"(qs0 + (unsigned)tm * (BK * N_ * 4)));
  SCHED();
  asm volatile("" :: "s"(x), "s"(qw), "s"(cb), "s"(am), "s"(ws), "s"(out));
  gu32* flags = (gu32*)(ws + SYNC_OFF + (size_t)tn * 2048);
  typedef __attribute__((address_space(3))) unsigned lu32;
  lu32* cnt = (lu32*)(shm + CNT_OFF);
#define SIGNAL(v_) asm volatile("s_mov_b64 exec, 1\n\tds_add_u32 %0, %1\n\ts_mov_b64 exec, -1" :: "v"(cnt), "v"(v_) : "memory")
#define AWAIT(m_, n_) do { while ((__hip_atomic_load(cnt, __ATOMIC_RELAXED, __HIP_MEMORY_SCOPE_WORKGROUP) & (m_)) < (n_)) {}     \
      asm volatile("" ::: "memory"); SCHED(); } while (0)
  char* lut = shm + LUT_OFF;
  const __amdgpu_buffer_rsrc_t rsW = __builtin_amdgcn_make_buffer_rsrc((void*)ws, (short)0, (int)WIMG_BYTES, 0x00020000);
  const __amdgpu_buffer_rsrc_t rsQ = __builtin_amdgcn_make_buffer_rsrc((void*)qw, (short)0, K_ * N_ * 4, 0x00020000);
  const uint16_t* am0 = am + tn * BN;
  const unsigned zero = 0;
#define Q_ROW(Q, j, so_) asm volatile("buffer_load_dwordx4 %0, %1, %2, %3 offen" : "=&v"(Q.r[j]) : "v"(qvoff), "s"(rsQ), "s"((so_) + (j) * (N_ * 4)) : "memory")
#define Q_ROWS(Q, so_) do { _Pragma("unroll") for (int j_ = 0; j_ < 8; j_++) Q_ROW(Q, j_, so_); } while (0)
#define Q_SC(S, ks) asm volatile("global_load_dwordx2 %0, %1, %2" : "=&v"(S) : "v"(avoff), "s"(am0 + (ks) * N_) : "memory")
#define Q_SO(ks) (qs0 + (unsigned)(ks) * (BK * N_ * 4))
#define TAG_LOAD(T) asm volatile("global_load_dword %0, %1, %2 sc1" : "=&v"(T) : "v"(zero), "s"(flags) : "memory")
#define SCALES(S) do { asm volatile("" : "+v"(S));                                                                                \
      sc[0] = __builtin_bit_cast(float, S.x << 16); sc[1] = __builtin_bit_cast(float, S.x & 0xffff0000u);                         \
      sc[2] = __builtin_bit_cast(float, S.y << 16); sc[3] = __builtin_bit_cast(float, S.y & 0xffff0000u); } while (0)
#define R_LOOK(Q, a_, n_) do { WAIT_V(n_); asm volatile("" : "+v"(Q.r[2 * (a_)]), "+v"(Q.r[2 * (a_) + 1]));                        \
      _Pragma("unroll") for (int i_ = 0; i_ < 4; i_ += 2) { u32x2 d_;                                                             \
        asm("v_lshl_add_u64 %0, %1, 4, %2\n\tv_lshl_add_u64 %0, %0, 3, 0" : "=&v"(d_)                                            \
            : "v"((u32x2){(unsigned)Q.r[2 * (a_) + 1][i_], (unsigned)Q.r[2 * (a_) + 1][i_ + 1]}),                                 \
              "v"((u32x2){(unsigned)Q.r[2 * (a_)][i_], (unsigned)Q.r[2 * (a_)][i_ + 1]}));                                        \
        lv[i_][a_] = *(const f32x2*)(lut + d_.x); lv[i_ + 1][a_] = *(const f32x2*)(lut + d_.y); }                                 \
      SCHED(); } while (0)
#define R_PACK(a_, sm) do { _Pragma("unroll") for (int i_ = 0; i_ < 4; i_++)                                                      \
      po[i_][a_] = (sm) ? pack_bf16(smul(lv[i_][a_].x, sc[i_]), smul(lv[i_][a_].y, sc[i_])) : pack_bf16(lv[i_][a_].x * sc[i_], lv[i_][a_].y * sc[i_]); \
      SCHED(); } while (0)
#define R_WRITE(dst) do { _Pragma("unroll") for (int i_ = 0; i_ < 4; i_++) *(u32x4*)(shm + (dst) + (i_ < 2 ? io0 : io1) + i_ * 896) = po[i_]; SCHED(); } while (0)
#define ROUND(Q, S, dst, e, sm) do { R_LOOK(Q, 0, (e) + 6); SCALES(S); R_LOOK(Q, 1, (e) + 4); R_PACK(0, sm);                      \
      R_LOOK(Q, 2, (e) + 2); R_PACK(1, sm); R_LOOK(Q, 3, e); R_PACK(2, sm); R_PACK(3, sm); R_WRITE(dst); } while (0)
#define PUBLISH(src, ks, kcp) do { WAIT_L(0); if (lane < 56) {                                                                    \
        const unsigned g_ = (unsigned)((tn * NT + (ks)) * WB + (kcp) * (2 * KCB) + lane * 16); u32x4 pv_[4];                      \
        _Pragma("unroll") for (int i_ = 0; i_ < 4; i_++) pv_[i_] = *(const u32x4*)(shm + (src) + lane * 16 + i_ * 896);           \
        _Pragma("unroll") for (int i_ = 0; i_ < 4; i_++) __builtin_amdgcn_raw_buffer_store_b128(pv_[i_], rsW, g_ + i_ * 896, 0, AUX_SC1); } \
      SCHED(); } while (0)
  f32x2 lv[4][4]; float sc[4]; u32x4 po[4];
  const __amdgpu_buffer_rsrc_t rsO = __builtin_amdgcn_make_buffer_rsrc((void*)out, (short)0, M_ * N_ * 2, 0x00020000);
  const int f0 = (tn & 3) * 2;
  unsigned vo; int ro;
#define O_ADDR(whole) do { const int r8 = lane >> 3, cq = lane & 7;                                                               \
      vo = (unsigned)(((tm * BM + (wid & 3) * 64 + r8) * N_ + tn * BN) * 2 + ((whole) ? f0 + cq : cq < f0 ? cq : cq + 8) * 16);    \
      if (!(whole) && cq >= 6) vo = 0x7fffffffu;                                                                                  \
      ro = HDR + ((whole) ? SB : 0) + (wid & 3) * 8192 + r8 * 128 + ((cq ^ r8) << 4);                                             \
      asm volatile("" : "+v"(vo), "+v"(ro)); } while (0)
#define O_RD(i_) (*(const u32x4*)(shm_raw + ro + (i_) * 1024))
#define O_ST(v_, i_, aux) __builtin_amdgcn_raw_buffer_store_b128(v_, rsO, vo + (i_) * 8 * (N_ * 2), 0, aux)
#define IO_OF() const int cg_ = nc >> 1, g16 = (cg_ < f0 ? cg_ + 8 : cg_ < f0 + 8 ? cg_ - f0 : cg_) * 32 + (nc & 1) * 16;           \
      const int io0 = g16 + (lane >> 5) * 448, io1 = g16 + 448 - (lane >> 5) * 448

  if (__builtin_expect(wid < 4, 1)) {
    QRegs q; u32x2 qsv; unsigned c0, c1, tagv, xt0;
    asm volatile("global_load_ushort %0, %2, %4\n\tglobal_load_ushort %1, %3, %4" : "=&v"(c0), "=&v"(c1)
                 : "v"((unsigned)((lane & 15) * 2)), "v"((unsigned)((wid * 4 + (lane >> 4)) * 2)), "s"(cb) : "memory");
    TAG_LOAD(tagv);
    Q_SC(qsv, tm);
    asm volatile("global_load_dword %0, %1, %2" : "=&v"(xt0) : "v"(xtv), "s"(x) : "memory");
    Q_ROWS(q, Q_SO(tm));
    SCHED();
    IO_OF();
    const unsigned gb = (unsigned)((tn * NT + tm) * WB + kc0 * KCB);
    unsigned go0 = qact ? gb + io0 : 0x7fffffffu, go1 = qact ? gb + io1 : 0x7fffffffu;
    asm volatile("" : "+v"(go0), "+v"(go1));
    f32x4 acc[4][7];
#pragma unroll
    for (int m = 0; m < 4; m++)
#pragma unroll
      for (int n = 0; n < 7; n++) { acc[m][n] = (f32x4){0.f, 0.f, 0.f, 0.f}; asm volatile("" : "+v"(acc[m][n])); }
    WAIT_V(11);
    asm volatile("" : "+v"(c0), "+v"(c1)); asm volatile("" :: "v"(ct));
    *cnt = 0;
    *(u32x2*)(lut + tid * 8) = (u32x2){c0 << 16, c1 << 16};
    int fa = FLG + wid * 320 + lane * 4;
    asm volatile("" : "+v"(fa));
    *(unsigned*)(shm_raw + fa + 32) = 0u;
    WAIT_L(0);
    BAR();
    ROUND(q, qsv, XB + kc0 * KCB, 0, 0);
    WAIT_L(0);
    SIGNAL(1u);
    SCHED();
    __builtin_amdgcn_raw_buffer_store_b128(po[0], rsW, go0, 0, AUX_SC1);
    __builtin_amdgcn_raw_buffer_store_b128(po[1], rsW, go0 + 896, 0, AUX_SC1);
    __builtin_amdgcn_raw_buffer_store_b128(po[2], rsW, go1 + 2 * 896, 0, AUX_SC1);
    __builtin_amdgcn_raw_buffer_store_b128(po[3], rsW, go1 + 3 * 896, 0, AUX_SC1);
    SCHED();
    const int fr = lane & 15, fq = lane >> 4;
    const int xo0 = (wid * 64 + fr) * 128 + ((fq ^ (fr >> 1)) << 4), xo1 = xo0 ^ 64;
    const int wo = XB + (fq >> 1) * (2 * KCB) + (fr & 3) * 896 + ((fq ^ (fr >> 1)) & 1) * 448 + (fr >> 2) * 16;
    bf16x8 xa[4], wa[7], xb[4], wb[7];
#define RD(xf, wf, aw_, ax_, hf) do {                                                                 \
      _Pragma("unroll") for (int n_ = 0; n_ < 7; n_++) wf[n_] = *(const bf16x8*)(shm + (aw_) + (hf) * (4 * KCB) + n_ * 64); \
      _Pragma("unroll") for (int m_ = 0; m_ < 4; m_++) xf[m_] = *(const bf16x8*)(shm + (ax_) + m_ * 2048); } while (0)
#define MMR(xf, wf, i0, i1) do { _Pragma("unroll") for (int i_ = (i0); i_ < (i1); i_++) {             \
      const int m_ = i_ / 7, n_ = m_ & 1 ? 6 - i_ % 7 : i_ % 7;                                       \
      acc[m_][n_] = __builtin_amdgcn_mfma_f32_16x16x32_bf16(wf[n_], xf[m_], acc[m_][n_], 0, 0, 0); } } while (0)
    O_ADDR(false);
    int e0 = HDR + wid * 8192 + fr * 128 + (((fq >> 1) ^ (fr & 7)) << 4) + (fq & 1) * 8, e1 = e0 ^ 32, e2 = e0 ^ 64, e3 = e0 ^ 96;
    asm volatile("" : "+v"(e0), "+v"(e1), "+v"(e2), "+v"(e3));
    AWAIT(15u, 6u);
#define INTER11() do { _Pragma("unroll") for (int i_ = 0; i_ < 11; i_++) { SGB(0x8, 2); SGB(0x100, 1); } } while (0)
#define STAGE(st) do { int aw_ = wo + (st) * SB; asm volatile("" : "+v"(aw_));                                                    \
      RD(xa, wa, aw_, xo0 + (st) * SB, 0);                                                                                        \
      SCHED();                                                                                                                    \
      RD(xb, wb, aw_, xo1 + (st) * SB, 1);                                                                                        \
      MMR(xa, wa, 0, 28);                                                                                                         \
      INTER11(); SGB(0x8, 6);                                                                                                     \
      SCHED();                                                                                                                    \
      if ((st) == 0) {                                                          \
        WAIT_V(0); asm volatile("" : "+v"(tagv) : "v"(xt0));                                                                      \
        if (lane == 0) __hip_atomic_store(flags + 256 + tm * 32 + wid, TAG_OF(tagv), RLX_AGENT); }                                \
      MMR(xb, wb, 0, 14);                                                                                                         \
      SCHED();                                                                                                                    \
      MMR(xb, wb, 14, 28);                                                                                                        \
      SCHED(); } while (0)
    STAGE(0);
    BAR();
    STAGE(1);
    asm volatile("s_nop 0\n\ts_nop 0");
    BAR();
    int aw = wo + STG_OFF - XB, a0 = xo0 + 2 * SB, a1 = xo1 + 2 * SB, so = 2 * SB;
    RD(xa, wa, aw, a0, 0);
    asm volatile("" : "+v"(aw), "+v"(a1));
#pragma unroll 1
    for (int t = 2; t < NT - 2; t++) {
      SCHED();
      RD(xb, wb, aw, a1, 1);
      MMR(xa, wa, 0, 27);
      so = so == 2 * SB ? 0 : so + SB;
      aw = wo + so; a0 = xo0 + so; a1 = xo1 + so;
      INTER11(); SGB(0x8, 1); SGB(0x4, 3); SGB(0x8, 1); SGB(0x2, 2); SGB(0x8, 1); SGB(0x2, 1); SGB(0x8, 2);
      asm volatile("" : "+v"(aw), "+v"(a0), "+v"(a1));
      LGKM0();
      MMR(xa, wa, 27, 28);
      SCHED();
      BAR();
      SCHED();
      RD(xa, wa, aw, a0, 0);
      MMR(xb, wb, 0, 28);
      INTER11(); SGB(0x8, 4); SGB(0x4, 2); SGB(0x8, 2);
    }
    SCHED();
#define LD(o_) (*(const bf16x8*)(shm + (o_)))
#define MF(m_, n_, xv, wf) do { asm volatile("v_mfma_f32_16x16x32_bf16 %0, %1, %2, %0" : "+v"(acc[m_][n_]) : "v"(wf[n_]), "v"(xv)); SCHED(); } while (0)
#define RX(xf, b_, m_) xf[m_] = LD((b_) + (m_) * 2048)
#define RW(wf, b_, hf, n_) wf[n_] = LD((b_) + (hf) * (4 * KCB) + (n_) * 64)
#define CV(m_, j_) pk[m_][j_] = pack_bf16(acc[m_][(j_) / 2][((j_) & 1) * 2], acc[m_][(j_) / 2][((j_) & 1) * 2 + 1])
#define WL(n_) do { SCHED(); __builtin_amdgcn_s_waitcnt(0xC07F | ((n_) << 8)); SCHED(); } while (0)
#define W64(m_, n_) *(u32x2*)(shm_raw + (((n_) & 3) == 0 ? e0 : ((n_) & 3) == 1 ? e1 : ((n_) & 3) == 2 ? e2 : e3) + ((n_) < 4 ? SB : 0) + (m_) * 2048) = (u32x2){pk[m_][2 * (n_)], pk[m_][2 * (n_) + 1]}
#define FLAG(u_) do { asm volatile("" ::: "memory"); *(int*)(shm_raw + fa + 4 * (u_)) = fa; asm volatile("" ::: "memory"); } while (0)
    {
      bf16x8 xc[4], xd[4], wc[7], wd[7];
      unsigned pk[4][14];
      u32x4 ov[8];
      MF(0, 0, xa[0], wa); RX(xb, a1, 0); SCHED();
      MF(0, 1, xa[0], wa); RW(wb, aw, 1, 0); SCHED();
      MF(0, 2, xa[0], wa); RW(wb, aw, 1, 1); SCHED();
      MF(0, 3, xa[0], wa); RW(wb, aw, 1, 2); SCHED();
      MF(1, 0, xa[1], wa); RW(wb, aw, 1, 3); SCHED();
      MF(1, 1, xa[1], wa); RX(xb, a1, 1); SCHED();
      MF(1, 2, xa[1], wa);
      BAR();
      SCHED();
      MF(1, 3, xa[1], wa); RX(xc, xo0, 0); SCHED();
      MF(2, 0, xa[2], wa); RW(wc, wo, 0, 0); SCHED();
      MF(2, 1, xa[2], wa); RX(xd, xo1, 0); SCHED();
      MF(2, 2, xa[2], wa); RW(wd, wo, 1, 0); SCHED();
      MF(2, 3, xa[2], wa); RW(wc, wo, 0, 1); SCHED();
      MF(3, 0, xa[3], wa); RW(wd, wo, 1, 1); SCHED();
      MF(3, 1, xa[3], wa); RW(wc, wo, 0, 2); SCHED();
      MF(3, 2, xa[3], wa); RW(wd, wo, 1, 2); SCHED();
      MF(3, 3, xa[3], wa); RW(wc, wo, 0, 3); WL(5); SCHED();
      MF(0, 0, xb[0], wb); RW(wd, wo, 1, 3); SCHED();
      MF(0, 0, xc[0], wc); RX(xc, xo0, 1); SCHED();
      MF(0, 0, xd[0], wd); RX(xd, xo1, 1); SCHED();
      MF(0, 1, xb[0], wb); WL(4); SCHED();
      MF(0, 1, xc[0], wc);
      MF(0, 1, xd[0], wd); CV(0, 0); CV(0, 1); SCHED();
      MF(0, 2, xb[0], wb); W64(0, 0); SCHED();
      MF(0, 2, xc[0], wc); RX(xb, a1, 2); WL(2); SCHED();
      MF(0, 2, xd[0], wd); CV(0, 2); CV(0, 3); SCHED();
      MF(0, 3, xb[0], wb); W64(0, 1); SCHED();
      MF(0, 3, xc[0], wc); RX(xc, xo0, 2); RX(xd, xo1, 2); SCHED();
      MF(0, 3, xd[0], wd); CV(0, 4); CV(0, 5); SCHED();
      MF(1, 0, xb[1], wb); W64(0, 2); SCHED();
      MF(1, 0, xc[1], wc); RX(xb, a1, 3); RX(xc, xo0, 3); SCHED();
      MF(1, 0, xd[1], wd); CV(0, 6); CV(0, 7); SCHED();
      MF(1, 1, xb[1], wb); W64(0, 3); SCHED();
      MF(1, 1, xc[1], wc); FLAG(1); SCHED();
      MF(1, 1, xd[1], wd); CV(1, 0); CV(1, 1); SCHED();
      MF(1, 2, xb[1], wb); W64(1, 0); SCHED();
      MF(1, 2, xc[1], wc); RX(xd, xo1, 3); SCHED();
      MF(1, 2, xd[1], wd); CV(1, 2); CV(1, 3); SCHED();
      MF(1, 3, xb[1], wb); W64(1, 1); SCHED();
      MF(1, 3, xc[1], wc); RW(wb, aw, 1, 4); WL(6); SCHED();
      MF(1, 3, xd[1], wd); CV(1, 4); CV(1, 5); SCHED();
      MF(2, 0, xb[2], wb); W64(1, 2); SCHED();
      MF(2, 0, xc[2], wc); RW(wc, wo, 0, 4); RW(wd, wo, 1, 4); SCHED();
      MF(2, 0, xd[2], wd); CV(1, 6); CV(1, 7); SCHED();
      MF(2, 1, xb[2], wb); W64(1, 3); SCHED();
      MF(2, 1, xc[2], wc); FLAG(2); SCHED();
      MF(2, 1, xd[2], wd); CV(2, 0); CV(2, 1); SCHED();
      MF(2, 2, xb[2], wb); W64(2, 0); SCHED();
      MF(2, 2, xc[2], wc); RW(wb, aw, 1, 5); WL(4); SCHED();
      MF(2, 2, xd[2], wd); CV(2, 2); CV(2, 3); SCHED();
      MF(2, 3, xb[2], wb); W64(2, 1); SCHED();
      MF(2, 3, xc[2], wc); RW(wc, wo, 0, 5); RW(wd, wo, 1, 5); SCHED();
      MF(2, 3, xd[2], wd); CV(2, 4); CV(2, 5); SCHED();
      MF(3, 0, xb[3], wb); W64(2, 2); SCHED();
      MF(3, 0, xc[3], wc); RW(wb, aw, 1, 6); RW(wc, wo, 0, 6); SCHED();
      MF(3, 0, xd[3], wd); CV(2, 6); CV(2, 7); SCHED();
      MF(3, 1, xb[3], wb); W64(2, 3); SCHED();
      MF(3, 1, xc[3], wc); FLAG(3); SCHED();
      MF(3, 1, xd[3], wd); CV(3, 0); CV(3, 1); SCHED();
      MF(3, 2, xb[3], wb); W64(3, 0); SCHED();
      MF(3, 2, xc[3], wc); RW(wd, wo, 1, 6); SCHED();
      MF(3, 2, xd[3], wd); CV(3, 2); CV(3, 3); SCHED();
      MF(3, 3, xb[3], wb); W64(3, 1); SCHED();
      MF(3, 3, xc[3], wc); WL(5); SCHED();
      MF(3, 3, xd[3], wd); CV(3, 4); CV(3, 5); SCHED();
      MF(0, 4, xa[0], wa); W64(3, 2); SCHED();
      MF(0, 4, xb[0], wb);
      MF(0, 4, xc[0], wc); CV(3, 6); CV(3, 7); SCHED();
      MF(0, 4, xd[0], wd); W64(3, 3); SCHED();
      MF(0, 5, xa[0], wa); FLAG(4); SCHED();
      MF(0, 5, xb[0], wb);
      MF(0, 5, xc[0], wc); CV(0, 8); CV(0, 9); SCHED();
      MF(0, 5, xd[0], wd); W64(0, 4); SCHED();
      MF(0, 6, xa[0], wa); WL(5); SCHED();
      MF(0, 6, xb[0], wb);
      MF(0, 6, xc[0], wc); CV(0, 10); CV(0, 11); SCHED();
      MF(0, 6, xd[0], wd); W64(0, 5); SCHED();
      MF(1, 4, xa[1], wa);
      MF(1, 4, xb[1], wb);
      MF(1, 4, xc[1], wc); CV(0, 12); CV(0, 13); SCHED();
      MF(1, 4, xd[1], wd); W64(0, 6); SCHED();
      MF(1, 5, xa[1], wa); FLAG(5); SCHED();
      MF(1, 5, xb[1], wb);
      MF(1, 5, xc[1], wc); CV(1, 8); CV(1, 9); SCHED();
      MF(1, 5, xd[1], wd); W64(1, 4); SCHED();
      MF(1, 6, xa[1], wa);
      MF(1, 6, xb[1], wb);
      MF(1, 6, xc[1], wc); CV(1, 10); CV(1, 11); SCHED();
      MF(1, 6, xd[1], wd); W64(1, 5); SCHED();
      MF(2, 4, xa[2], wa);
      MF(2, 4, xb[2], wb);
      MF(2, 4, xc[2], wc); CV(1, 12); CV(1, 13); SCHED();
      MF(2, 4, xd[2], wd); W64(1, 6); SCHED();
      MF(2, 5, xa[2], wa); FLAG(6); SCHED();
      MF(2, 5, xb[2], wb);
      MF(2, 5, xc[2], wc); CV(2, 8); CV(2, 9); SCHED();
      MF(2, 5, xd[2], wd); W64(2, 4); SCHED();
      MF(2, 6, xa[2], wa);
      MF(2, 6, xb[2], wb);
      MF(2, 6, xc[2], wc); CV(2, 10); CV(2, 11); SCHED();
      MF(2, 6, xd[2], wd); W64(2, 5); SCHED();
      MF(3, 4, xa[3], wa);
      MF(3, 4, xb[3], wb);
      MF(3, 4, xc[3], wc); CV(2, 12); CV(2, 13); SCHED();
      MF(3, 4, xd[3], wd); W64(2, 6); SCHED();
      MF(3, 5, xa[3], wa); ov[4] = O_RD(4); ov[5] = O_RD(5); SCHED();
      MF(3, 5, xb[3], wb);
      MF(3, 5, xc[3], wc); CV(3, 8); CV(3, 9); SCHED();
      MF(3, 5, xd[3], wd); W64(3, 4); SCHED();
      MF(3, 6, xa[3], wa);
      MF(3, 6, xb[3], wb);
      MF(3, 6, xc[3], wc); CV(3, 10); CV(3, 11); SCHED();
      MF(3, 6, xd[3], wd); W64(3, 5); SCHED();
      asm volatile("s_nop 7");
      SCHED();
      CV(3, 12); CV(3, 13); W64(3, 6); SCHED();
      ov[6] = O_RD(6); ov[7] = O_RD(7); SCHED();
      O_ST(ov[4], 4, 0); O_ST(ov[5], 5, 0); O_ST(ov[6], 6, 0); O_ST(ov[7], 7, 0);
    }
  } else {
    IO_OF();
    const unsigned long long ord = order_of(tm, tn);
#define KS(s_) ((int)(ord >> (4 * (s_))) & 15)
    const int h = wid & 1;
    unsigned xv[16];
#pragma unroll
    for (int i = 0; i < 16; i++) xv[i] = (unsigned)((8 * i + (lane >> 3)) * (K_ * 2) + (((lane & 7) ^ ((i & 1) * 4 + (lane >> 4))) << 4));
    const char* xbase = (const char*)(x + (size_t)(tm * BM + h * 128) * K_);
    const unsigned ldsx = (unsigned)(size_t)(shm) + h * 16384;
#define X8(o, l_, b_)                                                                                                             \
      asm volatile("s_mov_b32 m0, %8\n\ts_nop 0\n\tglobal_load_lds_dwordx4 %0, %9\n\t"                                            \
                   "s_add_u32 m0, m0, 0x400\n\ts_nop 0\n\tglobal_load_lds_dwordx4 %1, %9\n\t"                                     \
                   "s_add_u32 m0, m0, 0x400\n\ts_nop 0\n\tglobal_load_lds_dwordx4 %2, %9\n\t"                                     \
                   "s_add_u32 m0, m0, 0x400\n\ts_nop 0\n\tglobal_load_lds_dwordx4 %3, %9\n\t"                                     \
                   "s_add_u32 m0, m0, 0x400\n\ts_nop 0\n\tglobal_load_lds_dwordx4 %4, %9\n\t"                                     \
                   "s_add_u32 m0, m0, 0x400\n\ts_nop 0\n\tglobal_load_lds_dwordx4 %5, %9\n\t"                                     \
                   "s_add_u32 m0, m0, 0x400\n\ts_nop 0\n\tglobal_load_lds_dwordx4 %6, %9\n\t"                                     \
                   "s_add_u32 m0, m0, 0x400\n\ts_nop 0\n\tglobal_load_lds_dwordx4 %7, %9"                                         \
                   :: "v"(xv[o]), "v"(xv[o + 1]), "v"(xv[o + 2]), "v"(xv[o + 3]), "v"(xv[o + 4]), "v"(xv[o + 5]), "v"(xv[o + 6]), "v"(xv[o + 7]), \
                      "s"(l_), "s"(b_) : "memory", "scc")
#define ISSUE_X(s, slot) do { const char* b_ = xbase + KS(s) * 128; const unsigned l_ = ldsx + (slot) * SB;                       \
      X8(0, l_, b_); X8(8, l_ + 8192, b_); } while (0)
    const int fj = (int)(ord >> (4 * min(lane >> 2, 12) + 12));
    const unsigned fvo = (unsigned)(1024 + (fj & 7) * 128 + (fj & 8) * 2 + (lane & 3) * 4);
  if (wid < 6) {
    QRegs qa, q; u32x2 sa, sb;
    Q_SC(sa, k1);
    Q_SC(sb, k2);
    BAR();
    ISSUE_X(0, 0);
    SIGNAL(0x1000u);
    Q_ROWS(qa, Q_SO(k1));
    WAIT_V(8);
    SIGNAL(1u);
    { const char* b_ = xbase + k1 * 128; const unsigned l_ = ldsx + SB; X8(0, l_, b_); X8(8, l_ + 8192, b_); }
    SIGNAL(16u);
    asm volatile("" :: "v"(ct));
    Q_ROWS(q, Q_SO(k2));
    ROUND(qa, sa, SB + XB + kc0 * KCB, 24, 1);
    WAIT_V(8);
    WAIT_L(0);
    BAR();
    ROUND(q, sb, STG_OFF + kc0 * KCB, 0, 1);
    ISSUE_X(2, 2);
    WAIT_L(0);
    int xslot = 0;
#pragma unroll 1
    for (int t = 0; t < NT - 2; t++) {
      if (t + 3 < NT) { ISSUE_X(t + 3, xslot); xslot = xslot == NS - 1 ? 0 : xslot + 1; WAIT_V(16); } else { WAIT_V(0); }
      BAR();
    }
  } else {
    QRegs q, qa; u32x2 sa, sb; unsigned tagv;
    Q_SC(sa, k1); Q_SC(sb, k2); TAG_LOAD(tagv);
    BAR();
    AWAIT(0xf000u, 0x2000u);
    Q_ROWS(qa, Q_SO(k1));
    ROUND(qa, sa, SB + XB + kc0 * KCB, 0, 1);
    AWAIT(0xf0u, 32u);
    Q_ROWS(q, Q_SO(k2));
    asm volatile("" :: "v"(ct));
    WAIT_L(0);
    asm volatile("" : "+v"(tagv));
    const unsigned tag = TAG_OF(tagv);
    unsigned fv = tagv;
    const unsigned fvo6 = wid == 6 ? fvo : 0u;
    BAR();
    asm volatile("global_load_dword %0, %1, %2 sc1" : "+v"(fv) : "v"(fvo6), "s"(flags) : "memory");
    ROUND(q, sb, STG_OFF + kc0 * KCB, 1, 1);
    WAIT_L(0);
   if (wid == 6) {
    const char* wbase = ws + (size_t)tn * NT * WB;
    const unsigned wv = (unsigned)(lane * 16), wv2 = wv + 4096, wv3 = wv + 8192, wv4 = wv + 12288;
    const unsigned ldsw = (unsigned)(size_t)(shm) + XB;
#define ISSUE_W(s, slot) do { const char* b_ = wbase + (size_t)KS(s) * WB; const unsigned l_ = ldsw + (slot) * SB;                \
      asm volatile("s_mov_b32 m0, %4\n\ts_nop 0\n\t"                                                                              \
                   "global_load_lds_dwordx4 %0, %5 sc1\n\tglobal_load_lds_dwordx4 %0, %5 offset:1024 sc1\n\t"                     \
                   "global_load_lds_dwordx4 %0, %5 offset:2048 sc1\n\tglobal_load_lds_dwordx4 %0, %5 offset:3072 sc1\n\t"         \
                   "s_add_u32 m0, m0, 0x1000\n\ts_nop 0\n\t"                                                                      \
                   "global_load_lds_dwordx4 %1, %5 sc1\n\tglobal_load_lds_dwordx4 %1, %5 offset:1024 sc1\n\t"                     \
                   "global_load_lds_dwordx4 %1, %5 offset:2048 sc1\n\tglobal_load_lds_dwordx4 %1, %5 offset:3072 sc1\n\t"         \
                   "s_add_u32 m0, m0, 0x1000\n\ts_nop 0\n\t"                                                                      \
                   "global_load_lds_dwordx4 %2, %5 sc1\n\tglobal_load_lds_dwordx4 %2, %5 offset:1024 sc1\n\t"                     \
                   "global_load_lds_dwordx4 %2, %5 offset:2048 sc1\n\tglobal_load_lds_dwordx4 %2, %5 offset:3072 sc1\n\t"         \
                   "s_add_u32 m0, m0, 0x1000\n\ts_nop 0\n\t"                                                                      \
                   "global_load_lds_dwordx4 %3, %5 sc1\n\tglobal_load_lds_dwordx4 %3, %5 offset:1024 sc1"                         \
                   :: "v"(wv), "v"(wv2), "v"(wv3), "v"(wv4), "s"(l_), "s"(b_) : "memory", "scc"); } while (0)
#define SET_RDY(f_) do { const unsigned long long b_ = __ballot((f_) != tag);                                                     \
      rdy = 2 + ((int)__builtin_ctzll(b_ | (1ull << 52)) >> 2); } while (0)
#define POLL_BLOCK() do { unsigned f_; asm volatile("global_load_dword %0, %1, %2 sc1\n\ts_waitcnt vmcnt(0)" : "=&v"(f_) : "v"(fvo), "s"(flags) : "memory"); \
      SET_RDY(f_); } while (0)
#define NEXT_W() do { nw++; wslot = wslot == NS - 1 ? 0 : wslot + 1; } while (0)
    int rdy = 2, nw = 3, wslot = 0; unsigned spins = 0;
    bool ppoll = true, preg = false;
#pragma unroll 1
    for (int t = 0; t < NT - 2; t++) {
      const int lim = t + 3 < NT ? t + 3 : NT - 1;
      if (preg) { WAIT_V(14); } else { WAIT_V(0); }
      asm volatile("" : "+v"(fv));
      if (ppoll) SET_RDY(fv);
      if (nw <= t + 2) {
#pragma unroll 1
        while (nw > rdy) { if (++spins > SPIN_MAX) { rdy = NT; break; } POLL_BLOCK(); }
        ISSUE_W(nw, wslot); NEXT_W();
      }
      ppoll = rdy < NT - 1 && rdy < t + 5;
      asm volatile("global_load_dword %0, %1, %2 sc1" : "+v"(fv) : "v"(ppoll ? fvo : zero), "s"(ppoll ? (const char*)flags : (const char*)am) : "memory");
      preg = nw <= lim && nw <= rdy;
      if (preg) { ISSUE_W(nw, wslot); NEXT_W(); WAIT_V(15); } else { WAIT_V(1); }
      BAR();
    }
    WAIT_V(0);
    asm volatile("" :: "v"(fv));
    if (tm == 0 && lane == 0) __hip_atomic_store(flags, tag, RLX_AGENT);
   } else {
#pragma unroll 1
    for (int t = 1; t < NT - 1; t++) {
      BAR();
      if (t == 2) {
#pragma unroll 1
        for (int j = 0; j < 4; j++) PUBLISH(STG_OFF + 2 * j * KCB, k2, j);
      }
      if (t == 3) {
        WAIT_V(0);
        if (lane < 4) __hip_atomic_store(flags + 256 + tm * 32 + 4 + lane, tag, RLX_AGENT);
      }
    }
    WAIT_V(0);
    asm volatile("" :: "v"(fv));
   }
  }
    unsigned fp = (unsigned)(size_t)shm_raw + FLG + (wid & 3) * 320 + 256;
#define UNIT(aux) do { unsigned f_; u32x4 o0_, o1_;                                                                               \
      do { asm volatile("ds_read_b32 %0, %3\n\tds_read_b128 %1, %4\n\tds_read_b128 %2, %4 offset:1024\n\ts_waitcnt lgkmcnt(0)"     \
                        : "=&v"(f_), "=&v"(o0_), "=&v"(o1_) : "v"(fp), "v"(ro) : "memory");                                       \
      } while (__builtin_amdgcn_readfirstlane(f_) == 0);                                                                          \
      O_ST(o0_, 0, aux); O_ST(o1_, 1, aux); vo += 16 * (N_ * 2); ro += 2048; fp += 4; } while (0)
    O_ADDR(false);
    const unsigned vb = vo; const int rb = ro;
    O_ADDR(true);
    { unsigned f_; do { asm volatile("ds_read_b32 %0, %1\n\ts_waitcnt lgkmcnt(0)" : "=v"(f_) : "v"(fp) : "memory"); } while (__builtin_amdgcn_readfirstlane(f_) == 0); }
#pragma unroll 1
    for (int u = 0; u < 4; u++) UNIT(AUX_NT);
    vo = vb; ro = rb;
#pragma unroll 1
    for (int u = 4; u < 6; u++) UNIT(0);
  }
  asm volatile("" ::: "memory");
}

extern "C" void kernel_launch(void* const* d_in, const int* in_sizes, int n_in, void* d_out, int out_size, void* d_ws,
                              size_t ws_size, hipStream_t stream) {
  nf4_gemm<<<256, 512, 0, stream>>>((const uint16_t*)d_in[0], (const int*)d_in[1], (const uint16_t*)d_in[2],
                                    (const uint16_t*)d_in[3], (char*)d_ws, (uint16_t*)d_out);
}
```

```cpp
#include <hip/hip_runtime.h>
#include <hip/hip_bf16.h>
#include <stdint.h>

typedef __attribute__((ext_vector_type(8))) short bf16x8;
typedef __attribute__((ext_vector_type(4))) float f32x4;
typedef __attribute__((ext_vector_type(4))) int i32x4;
typedef __attribute__((ext_vector_type(4))) unsigned u32x4;
typedef __attribute__((ext_vector_type(2))) unsigned u32x2;
typedef __attribute__((ext_vector_type(2))) float f32x2;
typedef __attribute__((ext_vector_type(2))) __bf16 bf16x2_t;
typedef __attribute__((address_space(1))) unsigned gu32;

constexpr int M_ = 2048, K_ = 1024, N_ = 3584;
constexpr int BM = 256, BN = 112, BK = 64, NT = K_ / BK;
constexpr int XB = BM * BK * 2;
constexpr int WB = BN * BK * 2;
constexpr int KCB = BN * 16;
constexpr int SB = XB + WB;
constexpr int NS = 3;
constexpr int HDR = 4096;
constexpr int LUT_OFF = -HDR;
constexpr int FLG = 2560;
constexpr int CNT_OFF = -HDR + 2304;
constexpr int STG_OFF = NS * SB;
constexpr int SHM_BYTES = HDR + NS * SB + WB;
constexpr size_t WIMG_BYTES = (size_t)32 * NT * WB;
constexpr size_t SYNC_OFF = (size_t)8 << 20;
constexpr unsigned SPIN_MAX = 1u << 17;
constexpr unsigned CODE_TOUCH_MAX = 13900;
constexpr int AUX_NT = 2, AUX_SC1 = 16;

#define WAIT_V(n) asm volatile("s_waitcnt vmcnt(%0)" ::"n"(n) : "memory")
#define WAIT_L(n) asm volatile("s_waitcnt lgkmcnt(%0)" ::"n"(n) : "memory")
#define BAR() do { asm volatile("" ::: "memory"); __builtin_amdgcn_s_barrier(); asm volatile("" ::: "memory"); } while (0)
#define SCHED() __builtin_amdgcn_sched_barrier(0)
#define SGB(m_, n_) __builtin_amdgcn_sched_group_barrier(m_, n_, 0)
#define LGKM0() do { SCHED(); __builtin_amdgcn_s_waitcnt(0xC07F); SCHED(); } while (0)
#define TAG_OF(e) ((e) * 0x9E3779B1u + 0x7F4A7C15u)
#define RLX_AGENT __ATOMIC_RELAXED, __HIP_MEMORY_SCOPE_AGENT

__device__ __forceinline__ unsigned pack_bf16(float a, float b) {
  f32x2 v = {a, b};
  bf16x2_t r = __builtin_convertvector(v, bf16x2_t);
  return __builtin_bit_cast(unsigned, r);
}
__device__ __forceinline__ unsigned long long order_of(int tm, int tn) {
  const int t = tn & 15;
  const unsigned long long a = ((0x765432765432ull >> (8 * (t % 3))) & 0xffffff) ^ ((t & 1) * 0x111111);
  const unsigned long long b = (0xFEDCBA9FEDCBA9ull >> (4 * (t % 7))) & 0xfffffff;
  return (0x810 | a << 12 | b << 36) ^ (tm * 0x1111111111111111ull);
}

struct QRegs { i32x4 r[8]; };
__device__ __forceinline__ float smul(float a, float b) { float r; asm("v_mul_f32 %0, %1, %2" : "=v"(r) : "v"(a), "v"(b)); return r; }

__global__ __launch_bounds__(512, 2) void nf4_gemm(const uint16_t* __restrict__ x, const int* __restrict__ qw,
                                                   const uint16_t* __restrict__ cb, const uint16_t* __restrict__ am,
                                                   char* __restrict__ ws, uint16_t* __restrict__ out) {
  __shared__ __attribute__((aligned(1024))) char shm_raw[SHM_BYTES];
  char* const shm = shm_raw + HDR;
  unsigned long long pc0; asm volatile("s_getpc_b64 %0" : "=s"(pc0));
  unsigned ct;
  asm volatile("global_load_dword %0, %1, %2" : "=&v"(ct) : "v"(min((unsigned)((threadIdx.x & 15) * 1024 + ((blockIdx.x >> 3) & 7) * 128), CODE_TOUCH_MAX)), "s"(pc0) : "memory");
  const int bid = blockIdx.x;
  const int xcd = bid & 7, bi = bid >> 3;
  const int tn = (xcd >> 2) * 16 + (bi & 15), tm = (xcd & 3) * 2 + ((bi >> 4) & 1);
  const int k1 = tm ^ 1, k2 = tm + 8;
  const int tid = threadIdx.x, lane = tid & 63;
  const int wid = __builtin_amdgcn_readfirstlane(tid >> 6);
  const bool qact = (lane & 31) < 28;
  const int nc = qact ? (lane & 31) : 27;
  const int kc0 = 2 * (wid & 3);
  const int tj = tn & 3, cb0 = (tn >> 2) * 448;
  const int cw = tj * 64, cs = 256 + tj * 32, cq = 384 + tj * 16;
  const int col = nc * 4 + (nc < 16 ? cw : nc < 24 ? cs - 64 : cq - 96);
  const unsigned qvoff = (unsigned)(((kc0 + (lane >> 5)) * 8 * N_ + col) * 4);
  const unsigned avoff = (unsigned)(col * 2);
  const unsigned qs0 = (unsigned)(cb0 * 4);
  const unsigned xtv = (unsigned)((lane & 3) * (K_ * 2) + (((0x810 >> (4 * min(lane >> 2, 2))) & 15) ^ tm) * 128 + (tm * BM + (tn & 15) * 16 + (wid & 3) * 4) * (K_ * 2));
  asm volatile("" :: "v"(qvoff), "v"(avoff), "v"(xtv));
  asm volatile("" :: "s"(qs0 + (unsigned)tm * (BK * N_ * 4)));
  SCHED();
  asm volatile("" :: "s"(x), "s"(qw), "s"(cb), "s"(am), "s"(ws), "s"(out));
  gu32* flags = (gu32*)(ws + SYNC_OFF + (size_t)tn * 2048);
  typedef __attribute__((address_space(3))) unsigned lu32;
  lu32* cnt = (lu32*)(shm + CNT_OFF);
#define SIGNAL(v_) asm volatile("s_mov_b64 exec, 1\n\tds_add_u32 %0, %1\n\ts_mov_b64 exec, -1" :: "v"(cnt), "v"(v_) : "memory")
#define AWAIT(m_, n_) do { while ((__hip_atomic_load(cnt, __ATOMIC_RELAXED, __HIP_MEMORY_SCOPE_WORKGROUP) & (m_)) < (n_)) {}     \
      asm volatile("" ::: "memory"); SCHED(); } while (0)
  char* lut = shm + LUT_OFF;
  const __amdgpu_buffer_rsrc_t rsW = __builtin_amdgcn_make_buffer_rsrc((void*)ws, (short)0, (int)WIMG_BYTES, 0x00020000);
  const __amdgpu_buffer_rsrc_t rsQ = __builtin_amdgcn_make_buffer_rsrc((void*)qw, (short)0, K_ * N_ * 4, 0x00020000);
  const uint16_t* am0 = am + cb0;
  const unsigned zero = 0;
#define Q_ROW(Q, j, so_) asm volatile("buffer_load_dwordx4 %0, %1, %2, %3 offen" : "=&v"(Q.r[j]) : "v"(qvoff), "s"(rsQ), "s"((so_) + (j) * (N_ * 4)) : "memory")
#define Q_ROWS(Q, so_) do { _Pragma("unroll") for (int j_ = 0; j_ < 8; j_++) Q_ROW(Q, j_, so_); } while (0)
#define Q_SC(S, ks) asm volatile("global_load_dwordx2 %0, %1, %2" : "=&v"(S) : "v"(avoff), "s"(am0 + (ks) * N_) : "memory")
#define Q_SO(ks) (qs0 + (unsigned)(ks) * (BK * N_ * 4))
#define TAG_LOAD(T) asm volatile("global_load_dword %0, %1, %2 sc1" : "=&v"(T) : "v"(zero), "s"(flags) : "memory")
#define SCALES(S) do { asm volatile("" : "+v"(S));                                                                                \
      sc[0] = __builtin_bit_cast(float, S.x << 16); sc[1] = __builtin_bit_cast(float, S.x & 0xffff0000u);                         \
      sc[2] = __builtin_bit_cast(float, S.y << 16); sc[3] = __builtin_bit_cast(float, S.y & 0xffff0000u); } while (0)
#define R_LOOK(Q, a_, n_) do { WAIT_V(n_); asm volatile("" : "+v"(Q.r[2 * (a_)]), "+v"(Q.r[2 * (a_) + 1]));                        \
      _Pragma("unroll") for (int i_ = 0; i_ < 4; i_ += 2) { u32x2 d_;                                                             \
        asm("v_lshl_add_u64 %0, %1, 4, %2\n\tv_lshl_add_u64 %0, %0, 3, 0" : "=&v"(d_)                                            \
            : "v"((u32x2){(unsigned)Q.r[2 * (a_) + 1][i_], (unsigned)Q.r[2 * (a_) + 1][i_ + 1]}),                                 \
              "v"((u32x2){(unsigned)Q.r[2 * (a_)][i_], (unsigned)Q.r[2 * (a_)][i_ + 1]}));                                        \
        lv[i_][a_] = *(const f32x2*)(lut + d_.x); lv[i_ + 1][a_] = *(const f32x2*)(lut + d_.y); }                                 \
      SCHED(); } while (0)
#define R_PACK(a_, sm) do { _Pragma("unroll") for (int i_ = 0; i_ < 4; i_++)                                                      \
      po[i_][a_] = (sm) ? pack_bf16(smul(lv[i_][a_].x, sc[i_]), smul(lv[i_][a_].y, sc[i_])) : pack_bf16(lv[i_][a_].x * sc[i_], lv[i_][a_].y * sc[i_]); \
      SCHED(); } while (0)
#define R_WRITE(dst) do { _Pragma("unroll") for (int i_ = 0; i_ < 4; i_++) *(u32x4*)(shm + (dst) + (i_ < 2 ? io0 : io1) + i_ * 896) = po[i_]; SCHED(); } while (0)
#define ROUND(Q, S, dst, e, sm) do { R_LOOK(Q, 0, (e) + 6); SCALES(S); R_LOOK(Q, 1, (e) + 4); R_PACK(0, sm);                      \
      R_LOOK(Q, 2, (e) + 2); R_PACK(1, sm); R_LOOK(Q, 3, e); R_PACK(2, sm); R_PACK(3, sm); R_WRITE(dst); } while (0)
#define PUBLISH(src, ks, kcp) do { WAIT_L(0); if (lane < 56) {                                                                    \
        const unsigned g_ = (unsigned)((tn * NT + (ks)) * WB + (kcp) * (2 * KCB) + lane * 16); u32x4 pv_[4];                      \
        _Pragma("unroll") for (int i_ = 0; i_ < 4; i_++) pv_[i_] = *(const u32x4*)(shm + (src) + lane * 16 + i_ * 896);           \
        _Pragma("unroll") for (int i_ = 0; i_ < 4; i_++) __builtin_amdgcn_raw_buffer_store_b128(pv_[i_], rsW, g_ + i_ * 896, 0, AUX_SC1); } \
      SCHED(); } while (0)
  f32x2 lv[4][4]; float sc[4]; u32x4 po[4];
  const __amdgpu_buffer_rsrc_t rsO = __builtin_amdgcn_make_buffer_rsrc((void*)out, (short)0, M_ * N_ * 2, 0x00020000);
#define O_ADDR(vo, ro, k) unsigned vo; int ro; do { const int r_ = lane >> (3 - (k)), c_ = lane & (7 >> (k));                     \
      vo = (unsigned)(((tm * BM + (wid & 3) * 64 + r_) * N_ + cb0 + ((k) == 0 ? cw : (k) == 1 ? cs : cq)) * 2 + c_ * 16);     \
      ro = HDR + ((k) ? 0 : SB) + (wid & 3) * 8192 + r_ * 128 + (((c_ + ((k) & 2) * 2) ^ (r_ & 7)) << 4);                         \
      asm volatile("" : "+v"(vo), "+v"(ro)); } while (0)
#define O_RD(ro, r_) (*(const u32x4*)(shm_raw + (ro) + (r_) * 128))
#define O_ST(v_, vo, r_, aux) __builtin_amdgcn_raw_buffer_store_b128(v_, rsO, (vo) + (r_) * (N_ * 2), 0, aux)
#define IO_OF() const int io0 = nc * 16 + (lane >> 5) * 448, io1 = nc * 16 + 448 - (lane >> 5) * 448

  if (__builtin_expect(wid < 4, 1)) {
    QRegs q; u32x2 qsv; unsigned c0, c1, tagv, xt0;
    asm volatile("global_load_ushort %0, %2, %4\n\tglobal_load_ushort %1, %3, %4" : "=&v"(c0), "=&v"(c1)
                 : "v"((unsigned)((lane & 15) * 2)), "v"((unsigned)((wid * 4 + (lane >> 4)) * 2)), "s"(cb) : "memory");
    TAG_LOAD(tagv);
    Q_SC(qsv, tm);
    asm volatile("global_load_dword %0, %1, %2" : "=&v"(xt0) : "v"(xtv), "s"(x) : "memory");
    Q_ROWS(q, Q_SO(tm));
    SCHED();
    IO_OF();
    const unsigned gb = (unsigned)((tn * NT + tm) * WB + kc0 * KCB);
    unsigned go0 = qact ? gb + io0 : 0x7fffffffu, go1 = qact ? gb + io1 : 0x7fffffffu;
    asm volatile("" : "+v"(go0), "+v"(go1));
    f32x4 acc[4][7];
#pragma unroll
    for (int m = 0; m < 4; m++)
#pragma unroll
      for (int n = 0; n < 7; n++) { acc[m][n] = (f32x4){0.f, 0.f, 0.f, 0.f}; asm volatile("" : "+v"(acc[m][n])); }
    WAIT_V(11);
    asm volatile("" : "+v"(c0), "+v"(c1)); asm volatile("" :: "v"(ct));
    *cnt = 0;
    *(u32x2*)(lut + tid * 8) = (u32x2){c0 << 16, c1 << 16};
    int fa = FLG + wid * 320 + lane * 4;
    asm volatile("" : "+v"(fa));
    *(unsigned*)(shm_raw + fa + 32) = 0u;
    WAIT_L(0);
    BAR();
    ROUND(q, qsv, XB + kc0 * KCB, 0, 0);
    WAIT_L(0);
    SIGNAL(1u);
    SCHED();
    __builtin_amdgcn_raw_buffer_store_b128(po[0], rsW, go0, 0, AUX_SC1);
    __builtin_amdgcn_raw_buffer_store_b128(po[1], rsW, go0 + 896, 0, AUX_SC1);
    __builtin_amdgcn_raw_buffer_store_b128(po[2], rsW, go1 + 2 * 896, 0, AUX_SC1);
    __builtin_amdgcn_raw_buffer_store_b128(po[3], rsW, go1 + 3 * 896, 0, AUX_SC1);
    SCHED();
    const int fr = lane & 15, fq = lane >> 4;
    const int xo0 = (wid * 64 + fr) * 128 + ((fq ^ (fr >> 1)) << 4), xo1 = xo0 ^ 64;
    const int wo = XB + (fq >> 1) * (2 * KCB) + (fr & 3) * 896 + ((fq ^ (fr >> 1)) & 1) * 448 + (fr >> 2) * 16;
    bf16x8 xa[4], wa[7], xb[4], wb[7];
#define RD(xf, wf, aw_, ax_, hf) do {                                                                 \
      _Pragma("unroll") for (int n_ = 0; n_ < 7; n_++) wf[n_] = *(const bf16x8*)(shm + (aw_) + (hf) * (4 * KCB) + n_ * 64); \
      _Pragma("unroll") for (int m_ = 0; m_ < 4; m_++) xf[m_] = *(const bf16x8*)(shm + (ax_) + m_ * 2048); } while (0)
#define MMR(xf, wf, i0, i1) do { _Pragma("unroll") for (int i_ = (i0); i_ < (i1); i_++) {             \
      const int m_ = i_ / 7, n_ = m_ & 1 ? 6 - i_ % 7 : i_ % 7;                                       \
      acc[m_][n_] = __builtin_amdgcn_mfma_f32_16x16x32_bf16(wf[n_], xf[m_], acc[m_][n_], 0, 0, 0); } } while (0)
    O_ADDR(vs, rs, 1); O_ADDR(vq, rq, 2);
    int e0 = HDR + wid * 8192 + fr * 128 + (((fq >> 1) ^ (fr & 7)) << 4) + (fq & 1) * 8, e1 = e0 ^ 32, e2 = e0 ^ 64, e3 = e0 ^ 96;
    asm volatile("" : "+v"(e0), "+v"(e1), "+v"(e2), "+v"(e3));
    AWAIT(15u, 6u);
#define INTER11() do { _Pragma("unroll") for (int i_ = 0; i_ < 11; i_++) { SGB(0x8, 2); SGB(0x100, 1); } } while (0)
#define STAGE(st) do { int aw_ = wo + (st) * SB; asm volatile("" : "+v"(aw_));                                                    \
      RD(xa, wa, aw_, xo0 + (st) * SB, 0);                                                                                        \
      SCHED();                                                                                                                    \
      RD(xb, wb, aw_, xo1 + (st) * SB, 1);                                                                                        \
      MMR(xa, wa, 0, 28);                                                                                                         \
      INTER11(); SGB(0x8, 6);                                                                                                     \
      SCHED();                                                                                                                    \
      if ((st) == 0) {                                                          \
        WAIT_V(0); asm volatile("" : "+v"(tagv) : "v"(xt0));                                                                      \
        if (lane == 0) __hip_atomic_store(flags + 256 + tm * 32 + wid, TAG_OF(tagv), RLX_AGENT); }                                \
      MMR(xb, wb, 0, 14);                                                                                                         \
      SCHED();                                                                                                                    \
      MMR(xb, wb, 14, 28);                                                                                                        \
      SCHED(); } while (0)
    STAGE(0);
    BAR();
    STAGE(1);
    asm volatile(".rept 5\n\ts_nop 0\n\t.endr");
    BAR();
    int aw = wo + STG_OFF - XB, a0 = xo0 + 2 * SB, a1 = xo1 + 2 * SB, so = 2 * SB;
    RD(xa, wa, aw, a0, 0);
    asm volatile("" : "+v"(aw), "+v"(a1));
#pragma unroll 1
    for (int t = 2; t < NT - 2; t++) {
      SCHED();
      RD(xb, wb, aw, a1, 1);
      MMR(xa, wa, 0, 27);
      so = so == 2 * SB ? 0 : so + SB;
      aw = wo + so; a0 = xo0 + so; a1 = xo1 + so;
      INTER11(); SGB(0x8, 1); SGB(0x4, 3); SGB(0x8, 1); SGB(0x2, 2); SGB(0x8, 1); SGB(0x2, 1); SGB(0x8, 2);
      asm volatile("" : "+v"(aw), "+v"(a0), "+v"(a1));
      LGKM0();
      MMR(xa, wa, 27, 28);
      SCHED();
      BAR();
      SCHED();
      RD(xa, wa, aw, a0, 0);
      MMR(xb, wb, 0, 28);
      INTER11(); SGB(0x8, 4); SGB(0x4, 2); SGB(0x8, 2);
    }
    SCHED();
#define LD(o_) (*(const bf16x8*)(shm + (o_)))
#define MF(m_, n_, xv, wf) do { asm volatile("v_mfma_f32_16x16x32_bf16 %0, %1, %2, %0" : "+v"(acc[m_][n_]) : "v"(wf[n_]), "v"(xv)); SCHED(); } while (0)
#define RX(xf, b_, m_) xf[m_] = LD((b_) + (m_) * 2048)
#define RW(wf, b_, hf, n_) wf[n_] = LD((b_) + (hf) * (4 * KCB) + (n_) * 64)
#define CV(m_, j_) pk[m_][j_] = pack_bf16(acc[m_][(j_) / 2][((j_) & 1) * 2], acc[m_][(j_) / 2][((j_) & 1) * 2 + 1])
#define WL(n_) do { SCHED(); __builtin_amdgcn_s_waitcnt(0xC07F | ((n_) << 8)); SCHED(); } while (0)
#define W64(m_, n_) *(u32x2*)(shm_raw + (((n_) & 3) == 0 ? e0 : ((n_) & 3) == 1 ? e1 : ((n_) & 3) == 2 ? e2 : e3) + ((n_) < 4 ? SB : 0) + (m_) * 2048) = (u32x2){pk[m_][2 * (n_)], pk[m_][2 * (n_) + 1]}
#define FLAG(u_) do { asm volatile("" ::: "memory"); *(int*)(shm_raw + fa + 4 * (u_)) = fa; asm volatile("" ::: "memory"); } while (0)
    {
      bf16x8 xc[4], xd[4], wc[7], wd[7];
      unsigned pk[4][14];
      u32x4 ov[3];
      MF(0, 0, xa[0], wa); RX(xb, a1, 0); SCHED();
      MF(0, 1, xa[0], wa); RW(wb, aw, 1, 0); SCHED();
      MF(0, 2, xa[0], wa); RW(wb, aw, 1, 1); SCHED();
      MF(0, 3, xa[0], wa); RW(wb, aw, 1, 2); SCHED();
      MF(1, 0, xa[1], wa); RW(wb, aw, 1, 3); SCHED();
      MF(1, 1, xa[1], wa); RX(xb, a1, 1); SCHED();
      MF(1, 2, xa[1], wa);
      BAR();
      SCHED();
      MF(1, 3, xa[1], wa); RX(xc, xo0, 0); SCHED();
      MF(2, 0, xa[2], wa); RW(wc, wo, 0, 0); SCHED();
      MF(2, 1, xa[2], wa); RX(xd, xo1, 0); SCHED();
      MF(2, 2, xa[2], wa); RW(wd, wo, 1, 0); SCHED();
      MF(2, 3, xa[2], wa); RW(wc, wo, 0, 1); SCHED();
      MF(3, 0, xa[3], wa); RW(wd, wo, 1, 1); SCHED();
      MF(3, 1, xa[3], wa); RW(wc, wo, 0, 2); SCHED();
      MF(3, 2, xa[3], wa); RW(wd, wo, 1, 2); SCHED();
      MF(3, 3, xa[3], wa); RW(wc, wo, 0, 3); WL(5); SCHED();
      MF(0, 0, xb[0], wb); RW(wd, wo, 1, 3); SCHED();
      MF(0, 0, xc[0], wc); RX(xc, xo0, 1); SCHED();
      MF(0, 0, xd[0], wd); RX(xd, xo1, 1); SCHED();
      MF(0, 1, xb[0], wb); WL(4); SCHED();
      MF(0, 1, xc[0], wc);
      MF(0, 1, xd[0], wd); CV(0, 0); CV(0, 1); SCHED();
      MF(0, 2, xb[0], wb); W64(0, 0); SCHED();
      MF(0, 2, xc[0], wc); RX(xb, a1, 2); WL(2); SCHED();
      MF(0, 2, xd[0], wd); CV(0, 2); CV(0, 3); SCHED();
      MF(0, 3, xb[0], wb); W64(0, 1); SCHED();
      MF(0, 3, xc[0], wc); RX(xc, xo0, 2); RX(xd, xo1, 2); SCHED();
      MF(0, 3, xd[0], wd); CV(0, 4); CV(0, 5); SCHED();
      MF(1, 0, xb[1], wb); W64(0, 2); SCHED();
      MF(1, 0, xc[1], wc); RX(xb, a1, 3); RX(xc, xo0, 3); SCHED();
      MF(1, 0, xd[1], wd); CV(0, 6); CV(0, 7); SCHED();
      MF(1, 1, xb[1], wb); W64(0, 3); SCHED();
      MF(1, 1, xc[1], wc); FLAG(1); SCHED();
      MF(1, 1, xd[1], wd); CV(1, 0); CV(1, 1); SCHED();
      MF(1, 2, xb[1], wb); W64(1, 0); SCHED();
      MF(1, 2, xc[1], wc); RX(xd, xo1, 3); SCHED();
      MF(1, 2, xd[1], wd); CV(1, 2); CV(1, 3); SCHED();
      MF(1, 3, xb[1], wb); W64(1, 1); SCHED();
      MF(1, 3, xc[1], wc); RW(wb, aw, 1, 4); WL(6); SCHED();
      MF(1, 3, xd[1], wd); CV(1, 4); CV(1, 5); SCHED();
      MF(2, 0, xb[2], wb); W64(1, 2); SCHED();
      MF(2, 0, xc[2], wc); RW(wc, wo, 0, 4); RW(wd, wo, 1, 4); SCHED();
      MF(2, 0, xd[2], wd); CV(1, 6); CV(1, 7); SCHED();
      MF(2, 1, xb[2], wb); W64(1, 3); SCHED();
      MF(2, 1, xc[2], wc); FLAG(2); SCHED();
      MF(2, 1, xd[2], wd); CV(2, 0); CV(2, 1); SCHED();
      MF(2, 2, xb[2], wb); W64(2, 0); SCHED();
      MF(2, 2, xc[2], wc); RW(wb, aw, 1, 5); WL(4); SCHED();
      MF(2, 2, xd[2], wd); CV(2, 2); CV(2, 3); SCHED();
      MF(2, 3, xb[2], wb); W64(2, 1); SCHED();
      MF(2, 3, xc[2], wc); RW(wc, wo, 0, 5); RW(wd, wo, 1, 5); SCHED();
      MF(2, 3, xd[2], wd); CV(2, 4); CV(2, 5); SCHED();
      MF(3, 0, xb[3], wb); W64(2, 2); SCHED();
      MF(3, 0, xc[3], wc); RW(wb, aw, 1, 6); RW(wc, wo, 0, 6); SCHED();
      MF(3, 0, xd[3], wd); CV(2, 6); CV(2, 7); SCHED();
      MF(3, 1, xb[3], wb); W64(2, 3); SCHED();
      MF(3, 1, xc[3], wc); FLAG(3); SCHED();
      MF(3, 1, xd[3], wd); CV(3, 0); CV(3, 1); SCHED();
      MF(3, 2, xb[3], wb); W64(3, 0); SCHED();
      MF(3, 2, xc[3], wc); RW(wd, wo, 1, 6); SCHED();
      MF(3, 2, xd[3], wd); CV(3, 2); CV(3, 3); SCHED();
      MF(3, 3, xb[3], wb); W64(3, 1); SCHED();
      MF(3, 3, xc[3], wc); WL(5); SCHED();
      MF(3, 3, xd[3], wd); CV(3, 4); CV(3, 5); SCHED();
      MF(0, 4, xa[0], wa); W64(3, 2); SCHED();
      MF(0, 4, xb[0], wb);
      MF(0, 4, xc[0], wc); CV(3, 6); CV(3, 7); SCHED();
      MF(0, 4, xd[0], wd); W64(3, 3); SCHED();
      MF(0, 5, xa[0], wa); FLAG(4); SCHED();
      MF(0, 5, xb[0], wb);
      MF(0, 5, xc[0], wc); CV(0, 8); CV(0, 9); SCHED();
      MF(0, 5, xd[0], wd); W64(0, 4); SCHED();
      MF(0, 6, xa[0], wa); WL(5); SCHED();
      MF(0, 6, xb[0], wb);
      MF(0, 6, xc[0], wc); CV(0, 10); CV(0, 11); SCHED();
      MF(0, 6, xd[0], wd); W64(0, 5); SCHED();
      MF(1, 4, xa[1], wa);
      MF(1, 4, xb[1], wb);
      MF(1, 4, xc[1], wc); CV(0, 12); CV(0, 13); SCHED();
      MF(1, 4, xd[1], wd); W64(0, 6); SCHED();
      MF(1, 5, xa[1], wa); FLAG(5); SCHED();
      MF(1, 5, xb[1], wb);
      MF(1, 5, xc[1], wc); CV(1, 8); CV(1, 9); SCHED();
      MF(1, 5, xd[1], wd); W64(1, 4); SCHED();
      MF(1, 6, xa[1], wa);
      MF(1, 6, xb[1], wb);
      MF(1, 6, xc[1], wc); CV(1, 10); CV(1, 11); SCHED();
      MF(1, 6, xd[1], wd); W64(1, 5); SCHED();
      MF(2, 4, xa[2], wa);
      MF(2, 4, xb[2], wb);
      MF(2, 4, xc[2], wc); CV(1, 12); CV(1, 13); SCHED();
      MF(2, 4, xd[2], wd); W64(1, 6); SCHED();
      MF(2, 5, xa[2], wa); FLAG(6); SCHED();
      MF(2, 5, xb[2], wb);
      MF(2, 5, xc[2], wc); CV(2, 8); CV(2, 9); SCHED();
      MF(2, 5, xd[2], wd); W64(2, 4); SCHED();
      MF(2, 6, xa[2], wa);
      MF(2, 6, xb[2], wb);
      MF(2, 6, xc[2], wc); CV(2, 10); CV(2, 11); SCHED();
      MF(2, 6, xd[2], wd); W64(2, 5); SCHED();
      MF(3, 4, xa[3], wa);
      MF(3, 4, xb[3], wb);
      MF(3, 4, xc[3], wc); CV(2, 12); CV(2, 13); SCHED();
      MF(3, 4, xd[3], wd); W64(2, 6); SCHED();
      MF(3, 5, xa[3], wa); ov[0] = O_RD(rs, 32); SCHED();
      MF(3, 5, xb[3], wb);
      MF(3, 5, xc[3], wc); CV(3, 8); CV(3, 9); SCHED();
      MF(3, 5, xd[3], wd); W64(3, 4); SCHED();
      MF(3, 6, xa[3], wa);
      MF(3, 6, xb[3], wb);
      MF(3, 6, xc[3], wc); CV(3, 10); CV(3, 11); SCHED();
      MF(3, 6, xd[3], wd); W64(3, 5); ov[1] = O_RD(rs, 48); SCHED();
      asm volatile("s_nop 6");
      SCHED();
      CV(3, 12); CV(3, 13); W64(3, 6); SCHED();
      ov[2] = O_RD(rq, 32); SCHED();
      O_ST(ov[0], vs, 32, AUX_NT); O_ST(ov[1], vs, 48, AUX_NT); O_ST(ov[2], vq, 32, 0);
    }
  } else {
    IO_OF();
    const unsigned long long ord = order_of(tm, tn);
#define KS(s_) ((int)(ord >> (4 * (s_))) & 15)
    const int h = wid & 1;
    unsigned xv[16];
#pragma unroll
    for (int i = 0; i < 16; i++) xv[i] = (unsigned)((8 * i + (lane >> 3)) * (K_ * 2) + (((lane & 7) ^ ((i & 1) * 4 + (lane >> 4))) << 4));
    const char* xbase = (const char*)(x + (size_t)(tm * BM + h * 128) * K_);
    const unsigned ldsx = (unsigned)(size_t)(shm) + h * 16384;
#define X8(o, l_, b_)                                                                                                             \
      asm volatile("s_mov_b32 m0, %8\n\ts_nop 0\n\tglobal_load_lds_dwordx4 %0, %9\n\t"                                            \
                   "s_add_u32 m0, m0, 0x400\n\ts_nop 0\n\tglobal_load_lds_dwordx4 %1, %9\n\t"                                     \
                   "s_add_u32 m0, m0, 0x400\n\ts_nop 0\n\tglobal_load_lds_dwordx4 %2, %9\n\t"                                     \
                   "s_add_u32 m0, m0, 0x400\n\ts_nop 0\n\tglobal_load_lds_dwordx4 %3, %9\n\t"                                     \
                   "s_add_u32 m0, m0, 0x400\n\ts_nop 0\n\tglobal_load_lds_dwordx4 %4, %9\n\t"                                     \
                   "s_add_u32 m0, m0, 0x400\n\ts_nop 0\n\tglobal_load_lds_dwordx4 %5, %9\n\t"                                     \
                   "s_add_u32 m0, m0, 0x400\n\ts_nop 0\n\tglobal_load_lds_dwordx4 %6, %9\n\t"                                     \
                   "s_add_u32 m0, m0, 0x400\n\ts_nop 0\n\tglobal_load_lds_dwordx4 %7, %9"                                         \
                   :: "v"(xv[o]), "v"(xv[o + 1]), "v"(xv[o + 2]), "v"(xv[o + 3]), "v"(xv[o + 4]), "v"(xv[o + 5]), "v"(xv[o + 6]), "v"(xv[o + 7]), \
                      "s"(l_), "s"(b_) : "memory", "scc")
#define ISSUE_X(s, slot) do { const char* b_ = xbase + KS(s) * 128; const unsigned l_ = ldsx + (slot) * SB;                       \
      X8(0, l_, b_); X8(8, l_ + 8192, b_); } while (0)
    const int fj = (int)(ord >> (4 * min(lane >> 2, 12) + 12));
    const unsigned fvo = (unsigned)(1024 + (fj & 7) * 128 + (fj & 8) * 2 + (lane & 3) * 4);
  if (wid < 6) {
    QRegs qa, q; u32x2 sa, sb;
    Q_SC(sa, k1);
    Q_SC(sb, k2);
    BAR();
    ISSUE_X(0, 0);
    SIGNAL(0x1000u);
    Q_ROWS(qa, Q_SO(k1));
    WAIT_V(8);
    SIGNAL(1u);
    { const char* b_ = xbase + k1 * 128; const unsigned l_ = ldsx + SB; X8(0, l_, b_); X8(8, l_ + 8192, b_); }
    SIGNAL(16u);
    asm volatile("" :: "v"(ct));
    Q_ROWS(q, Q_SO(k2));
    ROUND(qa, sa, SB + XB + kc0 * KCB, 24, 1);
    WAIT_V(8);
    WAIT_L(0);
    BAR();
    ROUND(q, sb, STG_OFF + kc0 * KCB, 0, 1);
    ISSUE_X(2, 2);
    WAIT_L(0);
    int xslot = 0;
#pragma unroll 1
    for (int t = 0; t < NT - 2; t++) {
      if (t + 3 < NT) { ISSUE_X(t + 3, xslot); xslot = xslot == NS - 1 ? 0 : xslot + 1; WAIT_V(16); } else { WAIT_V(0); }
      BAR();
    }
  } else {
    QRegs q, qa; u32x2 sa, sb; unsigned tagv;
    Q_SC(sa, k1); Q_SC(sb, k2); TAG_LOAD(tagv);
    BAR();
    AWAIT(0xf000u, 0x2000u);
    Q_ROWS(qa, Q_SO(k1));
    ROUND(qa, sa, SB + XB + kc0 * KCB, 0, 1);
    AWAIT(0xf0u, 32u);
    Q_ROWS(q, Q_SO(k2));
    asm volatile("" :: "v"(ct));
    WAIT_L(0);
    asm volatile("" : "+v"(tagv));
    const unsigned tag = TAG_OF(tagv);
    unsigned fv = tagv;
    const unsigned fvo6 = wid == 6 ? fvo : 0u;
    BAR();
    asm volatile("global_load_dword %0, %1, %2 sc1" : "+v"(fv) : "v"(fvo6), "s"(flags) : "memory");
    ROUND(q, sb, STG_OFF + kc0 * KCB, 1, 1);
    WAIT_L(0);
   if (wid == 6) {
    const char* wbase = ws + (size_t)tn * NT * WB;
    const unsigned wv = (unsigned)(lane * 16), wv2 = wv + 4096, wv3 = wv + 8192, wv4 = wv + 12288;
    const unsigned ldsw = (unsigned)(size_t)(shm) + XB;
#define ISSUE_W(s, slot) do { const char* b_ = wbase + (size_t)KS(s) * WB; const unsigned l_ = ldsw + (slot) * SB;                \
      asm volatile("s_mov_b32 m0, %4\n\ts_nop 0\n\t"                                                                              \
                   "global_load_lds_dwordx4 %0, %5 sc1\n\tglobal_load_lds_dwordx4 %0, %5 offset:1024 sc1\n\t"                     \
                   "global_load_lds_dwordx4 %0, %5 offset:2048 sc1\n\tglobal_load_lds_dwordx4 %0, %5 offset:3072 sc1\n\t"         \
                   "s_add_u32 m0, m0, 0x1000\n\ts_nop 0\n\t"                                                                      \
                   "global_load_lds_dwordx4 %1, %5 sc1\n\tglobal_load_lds_dwordx4 %1, %5 offset:1024 sc1\n\t"                     \
                   "global_load_lds_dwordx4 %1, %5 offset:2048 sc1\n\tglobal_load_lds_dwordx4 %1, %5 offset:3072 sc1\n\t"         \
                   "s_add_u32 m0, m0, 0x1000\n\ts_nop 0\n\t"                                                                      \
                   "global_load_lds_dwordx4 %2, %5 sc1\n\tglobal_load_lds_dwordx4 %2, %5 offset:1024 sc1\n\t"                     \
                   "global_load_lds_dwordx4 %2, %5 offset:2048 sc1\n\tglobal_load_lds_dwordx4 %2, %5 offset:3072 sc1\n\t"         \
                   "s_add_u32 m0, m0, 0x1000\n\ts_nop 0\n\t"                                                                      \
                   "global_load_lds_dwordx4 %3, %5 sc1\n\tglobal_load_lds_dwordx4 %3, %5 offset:1024 sc1"                         \
                   :: "v"(wv), "v"(wv2), "v"(wv3), "v"(wv4), "s"(l_), "s"(b_) : "memory", "scc"); } while (0)
#define SET_RDY(f_) do { const unsigned long long b_ = __ballot((f_) != tag);                                                     \
      rdy = 2 + ((int)__builtin_ctzll(b_ | (1ull << 52)) >> 2); } while (0)
#define POLL_BLOCK() do { unsigned f_; asm volatile("global_load_dword %0, %1, %2 sc1\n\ts_waitcnt vmcnt(0)" : "=&v"(f_) : "v"(fvo), "s"(flags) : "memory"); \
      SET_RDY(f_); } while (0)
#define NEXT_W() do { nw++; wslot = wslot == NS - 1 ? 0 : wslot + 1; } while (0)
    int rdy = 2, nw = 3, wslot = 0; unsigned spins = 0;
    bool ppoll = true, preg = false;
#pragma unroll 1
    for (int t = 0; t < NT - 2; t++) {
      const int lim = t + 3 < NT ? t + 3 : NT - 1;
      if (preg) { WAIT_V(14); } else { WAIT_V(0); }
      asm volatile("" : "+v"(fv));
      if (ppoll) SET_RDY(fv);
      if (nw <= t + 2) {
#pragma unroll 1
        while (nw > rdy) { if (++spins > SPIN_MAX) { rdy = NT; break; } POLL_BLOCK(); }
        ISSUE_W(nw, wslot); NEXT_W();
      }
      ppoll = rdy < NT - 1 && rdy < t + 5;
      asm volatile("global_load_dword %0, %1, %2 sc1" : "+v"(fv) : "v"(ppoll ? fvo : zero), "s"(ppoll ? (const char*)flags : (const char*)am) : "memory");
      preg = nw <= lim && nw <= rdy;
      if (preg) { ISSUE_W(nw, wslot); NEXT_W(); WAIT_V(15); } else { WAIT_V(1); }
      BAR();
    }
    WAIT_V(0);
    asm volatile("" :: "v"(fv));
    if (tm == 0 && lane == 0) __hip_atomic_store(flags, tag, RLX_AGENT);
   } else {
#pragma unroll 1
    for (int t = 1; t < NT - 1; t++) {
      BAR();
      if (t == 2) {
#pragma unroll 1
        for (int j = 0; j < 4; j++) PUBLISH(STG_OFF + 2 * j * KCB, k2, j);
      }
      if (t == 3) {
        WAIT_V(0);
        if (lane < 4) __hip_atomic_store(flags + 256 + tm * 32 + 4 + lane, tag, RLX_AGENT);
      }
    }
    WAIT_V(0);
    asm volatile("" :: "v"(fv));
   }
  }
    unsigned fp = (unsigned)(size_t)shm_raw + FLG + (wid & 3) * 320 + 256;
#define UNIT(a0_, a1_, o1_) u32x4 p0_, p1_; do { unsigned f_;                                                                         \
      do { asm volatile("ds_read_b32 %0, %3\n\tds_read_b128 %1, %4\n\tds_read_b128 %2, %5 offset:%6\n\ts_waitcnt lgkmcnt(0)"   \
                        : "=&v"(f_), "=&v"(p0_), "=&v"(p1_) : "v"(fp), "v"(a0_), "v"(a1_), "n"(o1_) : "memory");                  \
      } while (__builtin_amdgcn_readfirstlane(f_) == 0); fp += 4; } while (0)
    O_ADDR(vw, rw, 0); O_ADDR(vs, rs, 1); O_ADDR(vq, rq, 2);
    { unsigned f_; do { asm volatile("ds_read_b32 %0, %1\n\ts_waitcnt lgkmcnt(0)" : "=v"(f_) : "v"(fp) : "memory"); } while (__builtin_amdgcn_readfirstlane(f_) == 0); }
#pragma unroll 1
    for (int u = 0; u < 4; u++) { UNIT(rw, rw, 1024); O_ST(p0_, vw, 0, AUX_NT); O_ST(p1_, vw, 8, AUX_NT); vw += 16 * (N_ * 2); rw += 2048; }
    { UNIT(rs, rs, 0); O_ST(p0_, vs, 0, AUX_NT); }
    { UNIT(rq, rs, 2048); O_ST(p1_, vs, 16, AUX_NT); O_ST(p0_, vq, 0, 0); }
  }
  asm volatile("" ::: "memory");
}

extern "C" void kernel_launch(void* const* d_in, const int* in_sizes, int n_in, void* d_out, int out_size, void* d_ws,
                              size_t ws_size, hipStream_t stream) {
  nf4_gemm<<<256, 512, 0, stream>>>((const uint16_t*)d_in[0], (const int*)d_in[1], (const uint16_t*)d_in[2],
                                    (const uint16_t*)d_in[3], (char*)d_ws, (uint16_t*)d_out);
}
```

```cpp
#include <hip/hip_runtime.h>
#include <hip/hip_bf16.h>
#include <stdint.h>

typedef __attribute__((ext_vector_type(8))) short bf16x8;
typedef __attribute__((ext_vector_type(4))) float f32x4;
typedef __attribute__((ext_vector_type(4))) int i32x4;
typedef __attribute__((ext_vector_type(4))) unsigned u32x4;
typedef __attribute__((ext_vector_type(2))) unsigned u32x2;
typedef __attribute__((ext_vector_type(2))) float f32x2;
typedef __attribute__((ext_vector_type(2))) __bf16 bf16x2_t;
typedef __attribute__((address_space(1))) unsigned gu32;

constexpr int M_ = 2048, K_ = 1024, N_ = 3584;
constexpr int BM = 256, BN = 112, BK = 64, NT = K_ / BK;
constexpr int XB = BM * BK * 2;
constexpr int WB = BN * BK * 2;
constexpr int KCB = BN * 16;
constexpr int SB = XB + WB;
constexpr int NS = 3;
constexpr int HDR = 4096;
constexpr int LUT_OFF = -HDR;
constexpr int FLG = 2560;
constexpr int CNT_OFF = -HDR + 2304;
constexpr int STG_OFF = NS * SB;
constexpr int SHM_BYTES = HDR + NS * SB + WB;
constexpr size_t WIMG_BYTES = (size_t)32 * NT * WB;
constexpr size_t SYNC_OFF = (size_t)8 << 20;
constexpr unsigned SPIN_MAX = 1u << 17;
constexpr unsigned CODE_TOUCH_MAX = 14876;
constexpr int AUX_NT = 2, AUX_SC1 = 16;

#define WAIT_V(n) asm volatile("s_waitcnt vmcnt(%0)" ::"n"(n) : "memory")
#define WAIT_L(n) asm volatile("s_waitcnt lgkmcnt(%0)" ::"n"(n) : "memory")
#define BAR() do { asm volatile("" ::: "memory"); __builtin_amdgcn_s_barrier(); asm volatile("" ::: "memory"); } while (0)
#define SCHED() __builtin_amdgcn_sched_barrier(0)
#define SGB(m_, n_) __builtin_amdgcn_sched_group_barrier(m_, n_, 0)
#define LGKM0() do { SCHED(); __builtin_amdgcn_s_waitcnt(0xC07F); SCHED(); } while (0)
#define TAG_OF(e) ((e) * 0x9E3779B1u + 0x7F4A7C15u)
#define RLX_AGENT __ATOMIC_RELAXED, __HIP_MEMORY_SCOPE_AGENT

__device__ __forceinline__ unsigned pack_bf16(float a, float b) {
  f32x2 v = {a, b};
  bf16x2_t r = __builtin_convertvector(v, bf16x2_t);
  return __builtin_bit_cast(unsigned, r);
}
__device__ __forceinline__ unsigned long long order_of(int tm, int tn) {
  const int t = tn & 15;
  const unsigned long long a = ((0x765432765432ull >> (8 * (t % 3))) & 0xffffff) ^ ((t & 1) * 0x111111);
  const unsigned long long b = (0xFEDCBA9FEDCBA9ull >> (4 * (t % 7))) & 0xfffffff;
  return (0x810 | a << 12 | b << 36) ^ (tm * 0x1111111111111111ull);
}

struct QRegs { i32x4 r[8]; };
__device__ __forceinline__ float smul(float a, float b) { float r; asm("v_mul_f32 %0, %1, %2" : "=v"(r) : "v"(a), "v"(b)); return r; }

__global__ __launch_bounds__(512, 2) void nf4_gemm(const uint16_t* __restrict__ x, const int* __restrict__ qw,
                                                   const uint16_t* __restrict__ cb, const uint16_t* __restrict__ am,
                                                   char* __restrict__ ws, uint16_t* __restrict__ out) {
  __shared__ __attribute__((aligned(1024))) char shm_raw[SHM_BYTES];
  char* const shm = shm_raw + HDR;
  unsigned long long pc0; asm volatile("s_getpc_b64 %0" : "=s"(pc0));
  unsigned ct;
  asm volatile("global_load_dword %0, %1, %2" : "=&v"(ct) : "v"(min((unsigned)((threadIdx.x & 15) * 1024 + ((blockIdx.x >> 3) & 7) * 128), CODE_TOUCH_MAX)), "s"(pc0) : "memory");
  const int bid = blockIdx.x;
  const int xcd = bid & 7, bi = bid >> 3;
  const int tn = (xcd >> 2) * 16 + (bi & 15), tm = (xcd & 3) * 2 + ((bi >> 4) & 1);
  const int k1 = tm ^ 1, k2 = tm + 8;
  const int tid = threadIdx.x, lane = tid & 63;
  const int wid = __builtin_amdgcn_readfirstlane(tid >> 6);
  const bool qact = (lane & 31) < 28;
  const int nc = qact ? (lane & 31) : 27;
  const int kc0 = 2 * (wid & 3);
  const int tj = tn & 3, cb0 = (tn >> 2) * 448;
  const int cw = tj * 64, cs = 256 + tj * 32, cq = 384 + tj * 16;
  const int col = nc * 4 + (nc < 16 ? cw : nc < 24 ? cs - 64 : cq - 96);
  const unsigned qvoff = (unsigned)(((kc0 + (lane >> 5)) * 8 * N_ + col) * 4);
  const unsigned avoff = (unsigned)(col * 2);
  const unsigned qs0 = (unsigned)(cb0 * 4);
  const unsigned xtv = (unsigned)((lane & 3) * (K_ * 2) + (((0x810 >> (4 * min(lane >> 2, 2))) & 15) ^ tm) * 128 + (tm * BM + (tn & 15) * 16 + (wid & 3) * 4) * (K_ * 2));
  asm volatile("" :: "v"(qvoff), "v"(avoff), "v"(xtv));
  asm volatile("" :: "s"(qs0 + (unsigned)tm * (BK * N_ * 4)));
  SCHED();
  asm volatile("" :: "s"(x), "s"(qw), "s"(cb), "s"(am), "s"(ws), "s"(out));
  gu32* flags = (gu32*)(ws + SYNC_OFF + (size_t)tn * 2048);
  typedef __attribute__((address_space(3))) unsigned lu32;
  lu32* cnt = (lu32*)(shm + CNT_OFF);
#define SIGNAL(v_) asm volatile("s_mov_b64 exec, 1\n\tds_add_u32 %0, %1\n\ts_mov_b64 exec, -1" :: "v"(cnt), "v"(v_) : "memory")
#define AWAIT(m_, n_) do { while ((__hip_atomic_load(cnt, __ATOMIC_RELAXED, __HIP_MEMORY_SCOPE_WORKGROUP) & (m_)) < (n_)) {}     \
      asm volatile("" ::: "memory"); SCHED(); } while (0)
  char* lut = shm + LUT_OFF;
  const __amdgpu_buffer_rsrc_t rsW = __builtin_amdgcn_make_buffer_rsrc((void*)ws, (short)0, (int)WIMG_BYTES, 0x00020000);
  const __amdgpu_buffer_rsrc_t rsQ = __builtin_amdgcn_make_buffer_rsrc((void*)qw, (short)0, K_ * N_ * 4, 0x00020000);
  const uint16_t* am0 = am + cb0;
  const unsigned zero = 0;
#define Q_ROW(Q, j, so_) asm volatile("buffer_load_dwordx4 %0, %1, %2, %3 offen" : "=&v"(Q.r[j]) : "v"(qvoff), "s"(rsQ), "s"((so_) + (j) * (N_ * 4)) : "memory")
#define Q_ROWS(Q, so_) do { _Pragma("unroll") for (int j_ = 0; j_ < 8; j_++) Q_ROW(Q, j_, so_); } while (0)
#define Q_SC(S, ks) asm volatile("global_load_dwordx2 %0, %1, %2" : "=&v"(S) : "v"(avoff), "s"(am0 + (ks) * N_) : "memory")
#define Q_SO(ks) (qs0 + (unsigned)(ks) * (BK * N_ * 4))
#define TAG_LOAD(T) asm volatile("global_load_dword %0, %1, %2 sc1" : "=&v"(T) : "v"(zero), "s"(flags) : "memory")
#define SCALES(S) do { asm volatile("" : "+v"(S));                                                                                \
      sc[0] = __builtin_bit_cast(float, S.x << 16); sc[1] = __builtin_bit_cast(float, S.x & 0xffff0000u);                         \
      sc[2] = __builtin_bit_cast(float, S.y << 16); sc[3] = __builtin_bit_cast(float, S.y & 0xffff0000u); } while (0)
#define R_LOOK(Q, a_, n_) do { WAIT_V(n_); asm volatile("" : "+v"(Q.r[2 * (a_)]), "+v"(Q.r[2 * (a_) + 1]));                        \
      _Pragma("unroll") for (int i_ = 0; i_ < 4; i_ += 2) { u32x2 d_;                                                             \
        asm("v_lshl_add_u64 %0, %1, 4, %2\n\tv_lshl_add_u64 %0, %0, 3, 0" : "=&v"(d_)                                            \
            : "v"((u32x2){(unsigned)Q.r[2 * (a_) + 1][i_], (unsigned)Q.r[2 * (a_) + 1][i_ + 1]}),                                 \
              "v"((u32x2){(unsigned)Q.r[2 * (a_)][i_], (unsigned)Q.r[2 * (a_)][i_ + 1]}));                                        \
        lv[i_][a_] = *(const f32x2*)(lut + d_.x); lv[i_ + 1][a_] = *(const f32x2*)(lut + d_.y); }                                 \
      SCHED(); } while (0)
#define R_PACK(a_, sm) do { _Pragma("unroll") for (int i_ = 0; i_ < 4; i_++)                                                      \
      po[i_][a_] = (sm) ? pack_bf16(smul(lv[i_][a_].x, sc[i_]), smul(lv[i_][a_].y, sc[i_])) : pack_bf16(lv[i_][a_].x * sc[i_], lv[i_][a_].y * sc[i_]); \
      SCHED(); } while (0)
#define R_WRITE(dst) do { _Pragma("unroll") for (int i_ = 0; i_ < 4; i_++) *(u32x4*)(shm + (dst) + (i_ < 2 ? io0 : io1) + i_ * 896) = po[i_]; SCHED(); } while (0)
#define ROUND(Q, S, dst, e, sm) do { R_LOOK(Q, 0, (e) + 6); SCALES(S); R_LOOK(Q, 1, (e) + 4); R_PACK(0, sm);                      \
      R_LOOK(Q, 2, (e) + 2); R_PACK(1, sm); R_LOOK(Q, 3, e); R_PACK(2, sm); R_PACK(3, sm); R_WRITE(dst); } while (0)
#define PUBLISH(src, ks, kcp) do { WAIT_L(0); if (lane < 56) {                                                                    \
        const unsigned g_ = (unsigned)((tn * NT + (ks)) * WB + (kcp) * (2 * KCB) + lane * 16); u32x4 pv_[4];                      \
        _Pragma("unroll") for (int i_ = 0; i_ < 4; i_++) pv_[i_] = *(const u32x4*)(shm + (src) + lane * 16 + i_ * 896);           \
        _Pragma("unroll") for (int i_ = 0; i_ < 4; i_++) __builtin_amdgcn_raw_buffer_store_b128(pv_[i_], rsW, g_ + i_ * 896, 0, AUX_SC1); } \
      SCHED(); } while (0)
  f32x2 lv[4][4]; float sc[4]; u32x4 po[4];
  const __amdgpu_buffer_rsrc_t rsO = __builtin_amdgcn_make_buffer_rsrc((void*)out, (short)0, M_ * N_ * 2, 0x00020000);
#define O_ADDR(vo, ro, k) unsigned vo; int ro; do { const int r_ = lane >> (3 - (k)), c_ = lane & (7 >> (k));                     \
      vo = (unsigned)(((tm * BM + (wid & 3) * 64 + r_) * N_ + cb0 + ((k) == 0 ? cw : (k) == 1 ? cs : cq)) * 2 + c_ * 16);     \
      ro = HDR + ((k) ? 0 : SB) + (wid & 3) * 8192 + r_ * 128 + (((c_ + ((k) & 2) * 2) ^ (r_ & 7)) << 4);                         \
      asm volatile("" : "+v"(vo), "+v"(ro)); } while (0)
#define O_RD(ro, r_) (*(const u32x4*)(shm_raw + (ro) + (r_) * 128))
#define O_ST(v_, vo, r_, aux) __builtin_amdgcn_raw_buffer_store_b128(v_, rsO, (vo) + (r_) * (N_ * 2), 0, aux)
#define IO_OF() const int io0 = nc * 16 + (lane >> 5) * 448, io1 = nc * 16 + 448 - (lane >> 5) * 448

  if (__builtin_expect(wid < 4, 1)) {
    QRegs q; u32x2 qsv; unsigned c0, c1, tagv, xt0;
    asm volatile("global_load_ushort %0, %2, %4\n\tglobal_load_ushort %1, %3, %4" : "=&v"(c0), "=&v"(c1)
                 : "v"((unsigned)((lane & 15) * 2)), "v"((unsigned)((wid * 4 + (lane >> 4)) * 2)), "s"(cb) : "memory");
    TAG_LOAD(tagv);
    Q_SC(qsv, tm);
    asm volatile("global_load_dword %0, %1, %2" : "=&v"(xt0) : "v"(xtv), "s"(x) : "memory");
    Q_ROWS(q, Q_SO(tm));
    SCHED();
    IO_OF();
    const unsigned gb = (unsigned)((tn * NT + tm) * WB + kc0 * KCB);
    unsigned go0 = qact ? gb + io0 : 0x7fffffffu, go1 = qact ? gb + io1 : 0x7fffffffu;
    asm volatile("" : "+v"(go0), "+v"(go1));
    f32x4 acc[4][7];
#pragma unroll
    for (int m = 0; m < 4; m++)
#pragma unroll
      for (int n = 0; n < 7; n++) { acc[m][n] = (f32x4){0.f, 0.f, 0.f, 0.f}; asm volatile("" : "+v"(acc[m][n])); }
    WAIT_V(11);
    asm volatile("" : "+v"(c0), "+v"(c1)); asm volatile("" :: "v"(ct));
    *cnt = 0;
    *(u32x2*)(lut + tid * 8) = (u32x2){c0 << 16, c1 << 16};
    int fa = FLG + wid * 320 + lane * 4;
    asm volatile("" : "+v"(fa));
    *(unsigned*)(shm_raw + fa + 64) = 0u;
    WAIT_L(0);
    BAR();
    ROUND(q, qsv, XB + kc0 * KCB, 0, 0);
    WAIT_L(0);
    SIGNAL(1u);
    SCHED();
    __builtin_amdgcn_raw_buffer_store_b128(po[0], rsW, go0, 0, AUX_SC1);
    __builtin_amdgcn_raw_buffer_store_b128(po[1], rsW, go0 + 896, 0, AUX_SC1);
    __builtin_amdgcn_raw_buffer_store_b128(po[2], rsW, go1 + 2 * 896, 0, AUX_SC1);
    __builtin_amdgcn_raw_buffer_store_b128(po[3], rsW, go1 + 3 * 896, 0, AUX_SC1);
    SCHED();
    const int fr = lane & 15, fq = lane >> 4;
    const int xo0 = (wid * 64 + fr) * 128 + ((fq ^ (fr >> 1)) << 4), xo1 = xo0 ^ 64;
    const int wo = XB + (fq >> 1) * (2 * KCB) + (fr & 3) * 896 + ((fq ^ (fr >> 1)) & 1) * 448 + (fr >> 2) * 16;
    bf16x8 xa[4], wa[7], xb[4], wb[7];
#define RD(xf, wf, aw_, ax_, hf) do {                                                                 \
      _Pragma("unroll") for (int n_ = 0; n_ < 7; n_++) wf[n_] = *(const bf16x8*)(shm + (aw_) + (hf) * (4 * KCB) + n_ * 64); \
      _Pragma("unroll") for (int m_ = 0; m_ < 4; m_++) xf[m_] = *(const bf16x8*)(shm + (ax_) + m_ * 2048); } while (0)
#define MMR(xf, wf, i0, i1) do { _Pragma("unroll") for (int i_ = (i0); i_ < (i1); i_++) {             \
      const int m_ = i_ / 7, n_ = m_ & 1 ? 6 - i_ % 7 : i_ % 7;                                       \
      acc[m_][n_] = __builtin_amdgcn_mfma_f32_16x16x32_bf16(wf[n_], xf[m_], acc[m_][n_], 0, 0, 0); } } while (0)
    O_ADDR(vq, rq, 2);
    int e0 = HDR + wid * 8192 + fr * 128 + (((fq >> 1) ^ (fr & 7)) << 4) + (fq & 1) * 8, e1 = e0 ^ 32, e2 = e0 ^ 64, e3 = e0 ^ 96;
    asm volatile("" : "+v"(e0), "+v"(e1), "+v"(e2), "+v"(e3));
    AWAIT(15u, 6u);
#define INTER11() do { _Pragma("unroll") for (int i_ = 0; i_ < 11; i_++) { SGB(0x8, 2); SGB(0x100, 1); } } while (0)
#define STAGE(st) do { int aw_ = wo + (st) * SB; asm volatile("" : "+v"(aw_));                                                    \
      RD(xa, wa, aw_, xo0 + (st) * SB, 0);                                                                                        \
      SCHED();                                                                                                                    \
      RD(xb, wb, aw_, xo1 + (st) * SB, 1);                                                                                        \
      MMR(xa, wa, 0, 28);                                                                                                         \
      INTER11(); SGB(0x8, 6);                                                                                                     \
      SCHED();                                                                                                                    \
      if ((st) == 0) {                                                          \
        WAIT_V(0); asm volatile("" : "+v"(tagv) : "v"(xt0));                                                                      \
        if (lane == 0) __hip_atomic_store(flags + 256 + tm * 32 + wid, TAG_OF(tagv), RLX_AGENT); }                                \
      MMR(xb, wb, 0, 14);                                                                                                         \
      SCHED();                                                                                                                    \
      MMR(xb, wb, 14, 28);                                                                                                        \
      SCHED(); } while (0)
    STAGE(0);
    BAR();
    STAGE(1);
    asm volatile(".rept 2\n\ts_nop 0\n\t.endr");
    BAR();
    int aw = wo + STG_OFF - XB, a0 = xo0 + 2 * SB, a1 = xo1 + 2 * SB, so = 2 * SB;
    RD(xa, wa, aw, a0, 0);
    asm volatile("" : "+v"(aw), "+v"(a1));
#define HALF_A() do { SCHED();                                                                                                    \
      RD(xb, wb, aw, a1, 1);                                                                                                      \
      MMR(xa, wa, 0, 27);                                                                                                         \
      so = so == 2 * SB ? 0 : so + SB;                                                                                            \
      aw = wo + so; a0 = xo0 + so; a1 = xo1 + so;                                                                                 \
      INTER11(); SGB(0x8, 1); SGB(0x4, 3); SGB(0x8, 1); SGB(0x2, 2); SGB(0x8, 1); SGB(0x2, 1); SGB(0x8, 2);                       \
      asm volatile("" : "+v"(aw), "+v"(a0), "+v"(a1));                                                                            \
      LGKM0();                                                                                                                    \
      MMR(xa, wa, 27, 28);                                                                                                        \
      SCHED();                                                                                                                    \
      BAR();                                                                                                             \
      SCHED(); } while (0)
#pragma unroll 1
    for (int t = 2; t < NT - 3; t++) {
      HALF_A();
      RD(xa, wa, aw, a0, 0);
      MMR(xb, wb, 0, 28);
      INTER11(); SGB(0x8, 4); SGB(0x4, 2); SGB(0x8, 2);
    }
    HALF_A();
    SCHED();
#define LD(o_) (*(const bf16x8*)(shm + (o_)))
#define MF(m_, n_, xv, wf) do { asm volatile("v_mfma_f32_16x16x32_bf16 %0, %1, %2, %0" : "+v"(acc[m_][n_]) : "v"(wf[n_]), "v"(xv)); SCHED(); } while (0)
#define RX(xf, b_, m_) xf[m_] = LD((b_) + (m_) * 2048)
#define RW(wf, b_, hf, n_) wf[n_] = LD((b_) + (hf) * (4 * KCB) + (n_) * 64)
#define CV(m_, j_) pk[m_][j_] = pack_bf16(acc[m_][(j_) / 2][((j_) & 1) * 2], acc[m_][(j_) / 2][((j_) & 1) * 2 + 1])
#define WL(n_) do { SCHED(); __builtin_amdgcn_s_waitcnt(0xC07F | ((n_) << 8)); SCHED(); } while (0)
#define W64(m_, n_) *(u32x2*)(shm_raw + (((n_) & 3) == 0 ? e0 : ((n_) & 3) == 1 ? e1 : ((n_) & 3) == 2 ? e2 : e3) + ((n_) < 4 ? SB : 0) + (m_) * 2048) = (u32x2){pk[m_][2 * (n_)], pk[m_][2 * (n_) + 1]}
#define FLAG(u_) do { asm volatile("" ::: "memory"); *(int*)(shm_raw + fa + 4 * (u_)) = fa; asm volatile("" ::: "memory"); } while (0)
    {
      bf16x8 xc[4], xd[4], xe[4], wc[7], wd[7], we[7];
      unsigned pk[4][14];
      u32x4 ov[2];
      MF(0, 0, xb[0], wb); RX(xa, a0, 0); SCHED();
      MF(0, 1, xb[0], wb); RW(wa, aw, 0, 0); SCHED();
      MF(0, 2, xb[0], wb); RW(wa, aw, 0, 1); SCHED();
      MF(0, 3, xb[0], wb); RW(wa, aw, 0, 2); SCHED();
      MF(1, 0, xb[1], wb); RW(wa, aw, 0, 3); SCHED();
      MF(1, 1, xb[1], wb); RX(xa, a0, 1); SCHED();
      MF(1, 2, xb[1], wb); RX(xc, a1, 0); SCHED();
      MF(1, 3, xb[1], wb); RW(wc, aw, 1, 0); SCHED();
      MF(2, 0, xb[2], wb); RW(wc, aw, 1, 1); SCHED();
      MF(2, 1, xb[2], wb); RW(wc, aw, 1, 2); SCHED();
      MF(2, 2, xb[2], wb); RW(wc, aw, 1, 3); SCHED();
      MF(2, 3, xb[2], wb); RX(xc, a1, 1); SCHED();
      MF(3, 0, xb[3], wb);
      MF(3, 1, xb[3], wb); RX(xa, a0, 2); SCHED();
      MF(3, 2, xb[3], wb); RX(xc, a1, 2); SCHED();
      MF(3, 3, xb[3], wb); WL(3); SCHED();
      MF(0, 0, xa[0], wa);
      MF(0, 1, xa[0], wa);
      MF(0, 2, xa[0], wa);
      MF(0, 3, xa[0], wa);
      BAR();
      SCHED();
      MF(1, 0, xa[1], wa); RX(xd, xo0, 0); RW(wd, wo, 0, 0); SCHED();
      MF(1, 1, xa[1], wa); RX(xe, xo1, 0); RW(we, wo, 1, 0); SCHED();
      MF(1, 2, xa[1], wa); RW(wd, wo, 0, 1); RW(we, wo, 1, 1); SCHED();
      MF(1, 3, xa[1], wa); RW(wd, wo, 0, 2); RW(we, wo, 1, 2); SCHED();
      MF(0, 0, xc[0], wc); RW(wd, wo, 0, 3); RW(we, wo, 1, 3); SCHED();
      MF(0, 1, xc[0], wc); RX(xd, xo0, 1); RX(xe, xo1, 1); SCHED();
      MF(0, 2, xc[0], wc); RX(xd, xo0, 2); RX(xe, xo1, 2); SCHED();
      MF(0, 3, xc[0], wc); WL(8); SCHED();
      MF(0, 0, xd[0], wd);
      MF(0, 0, xe[0], we);
      MF(0, 1, xd[0], wd);
      MF(0, 1, xe[0], we); WL(0); SCHED();
      MF(0, 2, xd[0], wd); CV(0, 0); CV(0, 1); SCHED();
      MF(0, 2, xe[0], we); W64(0, 0); SCHED();
      MF(0, 3, xd[0], wd); CV(0, 2); CV(0, 3); SCHED();
      MF(0, 3, xe[0], we); W64(0, 1); SCHED();
      MF(1, 0, xc[1], wc); CV(0, 4); CV(0, 5); SCHED();
      MF(1, 0, xd[1], wd); W64(0, 2); SCHED();
      MF(1, 0, xe[1], we); CV(0, 6); CV(0, 7); SCHED();
      MF(1, 1, xc[1], wc); W64(0, 3); SCHED();
      MF(1, 1, xd[1], wd); FLAG(1); SCHED();
      MF(1, 1, xe[1], we); CV(1, 0); CV(1, 1); SCHED();
      MF(1, 2, xc[1], wc); W64(1, 0); SCHED();
      MF(1, 2, xd[1], wd); RX(xa, a0, 3); RX(xc, a1, 3); SCHED();
      MF(1, 2, xe[1], we); CV(1, 2); CV(1, 3); SCHED();
      MF(1, 3, xc[1], wc); W64(1, 1); SCHED();
      MF(1, 3, xd[1], wd); RX(xd, xo0, 3); RX(xe, xo1, 3); SCHED();
      MF(1, 3, xe[1], we); CV(1, 4); CV(1, 5); SCHED();
      MF(2, 0, xa[2], wa); W64(1, 2); SCHED();
      MF(2, 0, xc[2], wc); RW(wb, wo + SB, 1, 4); RW(wa, aw, 0, 4); SCHED();
      MF(2, 0, xd[2], wd); CV(1, 6); CV(1, 7); SCHED();
      MF(2, 0, xe[2], we); W64(1, 3); SCHED();
      MF(2, 1, xa[2], wa); FLAG(2); SCHED();
      MF(2, 1, xc[2], wc); RW(wc, aw, 1, 4); SCHED();
      MF(2, 1, xd[2], wd); CV(2, 0); CV(2, 1); SCHED();
      MF(2, 1, xe[2], we); W64(2, 0); SCHED();
      MF(2, 2, xa[2], wa); RW(wd, wo, 0, 4); SCHED();
      MF(2, 2, xc[2], wc);
      MF(2, 2, xd[2], wd); CV(2, 2); CV(2, 3); SCHED();
      MF(2, 2, xe[2], we); W64(2, 1); SCHED();
      MF(2, 3, xa[2], wa); RW(we, wo, 1, 4); SCHED();
      MF(2, 3, xc[2], wc); WL(2); SCHED();
      MF(2, 3, xd[2], wd); CV(2, 4); CV(2, 5); SCHED();
      MF(2, 3, xe[2], we); W64(2, 2); SCHED();
      MF(3, 0, xa[3], wa); RW(wb, wo + SB, 1, 5); RW(wa, aw, 0, 5); SCHED();
      MF(3, 0, xc[3], wc); RW(wc, aw, 1, 5); SCHED();
      MF(3, 0, xd[3], wd); CV(2, 6); CV(2, 7); SCHED();
      MF(3, 0, xe[3], we); W64(2, 3); SCHED();
      MF(3, 1, xa[3], wa); FLAG(3); SCHED();
      MF(3, 1, xc[3], wc); RW(wd, wo, 0, 5); RW(we, wo, 1, 5); SCHED();
      MF(3, 1, xd[3], wd); CV(3, 0); CV(3, 1); SCHED();
      MF(3, 1, xe[3], we); W64(3, 0); SCHED();
      MF(3, 2, xa[3], wa);
      MF(3, 2, xc[3], wc);
      MF(3, 2, xd[3], wd); CV(3, 2); CV(3, 3); SCHED();
      MF(3, 2, xe[3], we); W64(3, 1); SCHED();
      MF(3, 3, xa[3], wa);
      MF(3, 3, xc[3], wc);
      MF(3, 3, xd[3], wd); CV(3, 4); CV(3, 5); SCHED();
      MF(3, 3, xe[3], we); W64(3, 2); SCHED();
      MF(0, 4, xb[0], wb); RW(wb, wo + SB, 1, 6); SCHED();
      MF(0, 4, xa[0], wa); RW(wa, aw, 0, 6); WL(5); SCHED();
      MF(0, 4, xc[0], wc); CV(3, 6); CV(3, 7); SCHED();
      MF(0, 4, xd[0], wd); W64(3, 3); SCHED();
      MF(0, 4, xe[0], we); FLAG(4); SCHED();
      MF(0, 5, xb[0], wb); RW(wc, aw, 1, 6); RW(wd, wo, 0, 6); SCHED();
      MF(0, 5, xa[0], wa); RW(we, wo, 1, 6); SCHED();
      MF(0, 5, xc[0], wc); CV(0, 8); CV(0, 9); SCHED();
      MF(0, 5, xd[0], wd); W64(0, 4); SCHED();
      MF(0, 5, xe[0], we);
      MF(1, 4, xb[1], wb);
      MF(1, 4, xa[1], wa);
      MF(1, 4, xc[1], wc); CV(0, 10); CV(0, 11); SCHED();
      MF(1, 4, xd[1], wd); W64(0, 5); SCHED();
      MF(1, 4, xe[1], we); FLAG(5); SCHED();
      MF(1, 5, xb[1], wb);
      MF(1, 5, xa[1], wa);
      MF(1, 5, xc[1], wc); CV(1, 8); CV(1, 9); SCHED();
      MF(1, 5, xd[1], wd); W64(1, 4); SCHED();
      MF(1, 5, xe[1], we);
      MF(2, 4, xb[2], wb);
      MF(2, 4, xa[2], wa);
      MF(2, 4, xc[2], wc); CV(1, 10); CV(1, 11); SCHED();
      MF(2, 4, xd[2], wd); W64(1, 5); SCHED();
      MF(2, 4, xe[2], we); FLAG(6); SCHED();
      MF(2, 5, xb[2], wb);
      MF(2, 5, xa[2], wa);
      MF(2, 5, xc[2], wc); CV(2, 8); CV(2, 9); SCHED();
      MF(2, 5, xd[2], wd); W64(2, 4); SCHED();
      MF(2, 5, xe[2], we);
      MF(3, 4, xb[3], wb);
      MF(3, 4, xa[3], wa);
      MF(3, 4, xc[3], wc); CV(2, 10); CV(2, 11); SCHED();
      MF(3, 4, xd[3], wd); W64(2, 5); SCHED();
      MF(3, 4, xe[3], we); FLAG(7); SCHED();
      MF(3, 5, xb[3], wb);
      MF(3, 5, xa[3], wa);
      MF(3, 5, xc[3], wc); CV(3, 8); CV(3, 9); SCHED();
      MF(3, 5, xd[3], wd); W64(3, 4); SCHED();
      MF(3, 5, xe[3], we); WL(10); SCHED();
      MF(0, 6, xb[0], wb);
      MF(0, 6, xa[0], wa);
      MF(0, 6, xc[0], wc); CV(3, 10); CV(3, 11); SCHED();
      MF(0, 6, xd[0], wd); W64(3, 5); SCHED();
      MF(0, 6, xe[0], we); FLAG(8); SCHED();
      MF(1, 6, xb[1], wb);
      MF(1, 6, xa[1], wa);
      MF(1, 6, xc[1], wc); CV(0, 12); CV(0, 13); SCHED();
      MF(1, 6, xd[1], wd); W64(0, 6); SCHED();
      MF(1, 6, xe[1], we);
      MF(2, 6, xb[2], wb);
      MF(2, 6, xa[2], wa);
      MF(2, 6, xc[2], wc); CV(1, 12); CV(1, 13); SCHED();
      MF(2, 6, xd[2], wd); W64(1, 6); SCHED();
      MF(2, 6, xe[2], we); ov[0] = O_RD(rq, 0); SCHED();
      MF(3, 6, xb[3], wb);
      MF(3, 6, xa[3], wa);
      MF(3, 6, xc[3], wc); CV(2, 12); CV(2, 13); SCHED();
      MF(3, 6, xd[3], wd); W64(2, 6); SCHED();
      MF(3, 6, xe[3], we);
      O_ST(ov[0], vq, 0, 0); SCHED();
      asm volatile("s_nop 6");
      SCHED();
      CV(3, 12); CV(3, 13); W64(3, 6); ov[1] = O_RD(rq, 32); SCHED();
      O_ST(ov[1], vq, 32, 0);
    }
  } else {
    IO_OF();
    const unsigned long long ord = order_of(tm, tn);
#define KS(s_) ((int)(ord >> (4 * (s_))) & 15)
    const int h = wid & 1;
    unsigned xv[16];
#pragma unroll
    for (int i = 0; i < 16; i++) xv[i] = (unsigned)((8 * i + (lane >> 3)) * (K_ * 2) + (((lane & 7) ^ ((i & 1) * 4 + (lane >> 4))) << 4));
    const char* xbase = (const char*)(x + (size_t)(tm * BM + h * 128) * K_);
    const unsigned ldsx = (unsigned)(size_t)(shm) + h * 16384;
#define X8(o, l_, b_)                                                                                                             \
      asm volatile("s_mov_b32 m0, %8\n\ts_nop 0\n\tglobal_load_lds_dwordx4 %0, %9\n\t"                                            \
                   "s_add_u32 m0, m0, 0x400\n\ts_nop 0\n\tglobal_load_lds_dwordx4 %1, %9\n\t"                                     \
                   "s_add_u32 m0, m0, 0x400\n\ts_nop 0\n\tglobal_load_lds_dwordx4 %2, %9\n\t"                                     \
                   "s_add_u32 m0, m0, 0x400\n\ts_nop 0\n\tglobal_load_lds_dwordx4 %3, %9\n\t"                                     \
                   "s_add_u32 m0, m0, 0x400\n\ts_nop 0\n\tglobal_load_lds_dwordx4 %4, %9\n\t"                                     \
                   "s_add_u32 m0, m0, 0x400\n\ts_nop 0\n\tglobal_load_lds_dwordx4 %5, %9\n\t"                                     \
                   "s_add_u32 m0, m0, 0x400\n\ts_nop 0\n\tglobal_load_lds_dwordx4 %6, %9\n\t"                                     \
                   "s_add_u32 m0, m0, 0x400\n\ts_nop 0\n\tglobal_load_lds_dwordx4 %7, %9"                                         \
                   :: "v"(xv[o]), "v"(xv[o + 1]), "v"(xv[o + 2]), "v"(xv[o + 3]), "v"(xv[o + 4]), "v"(xv[o + 5]), "v"(xv[o + 6]), "v"(xv[o + 7]), \
                      "s"(l_), "s"(b_) : "memory", "scc")
#define ISSUE_X(s, slot) do { const char* b_ = xbase + KS(s) * 128; const unsigned l_ = ldsx + (slot) * SB;                       \
      X8(0, l_, b_); X8(8, l_ + 8192, b_); } while (0)
    const int fj = (int)(ord >> (4 * min(lane >> 2, 12) + 12));
    const unsigned fvo = (unsigned)(1024 + (fj & 7) * 128 + (fj & 8) * 2 + (lane & 3) * 4);
  if (wid < 6) {
    QRegs qa, q; u32x2 sa, sb;
    Q_SC(sa, k1);
    Q_SC(sb, k2);
    BAR();
    ISSUE_X(0, 0);
    SIGNAL(0x1000u);
    Q_ROWS(qa, Q_SO(k1));
    WAIT_V(8);
    SIGNAL(1u);
    { const char* b_ = xbase + k1 * 128; const unsigned l_ = ldsx + SB; X8(0, l_, b_); X8(8, l_ + 8192, b_); }
    SIGNAL(16u);
    asm volatile("" :: "v"(ct));
    Q_ROWS(q, Q_SO(k2));
    ROUND(qa, sa, SB + XB + kc0 * KCB, 24, 1);
    WAIT_V(8);
    WAIT_L(0);
    BAR();
    ROUND(q, sb, STG_OFF + kc0 * KCB, 0, 1);
    ISSUE_X(2, 2);
    WAIT_L(0);
    int xslot = 0;
#pragma unroll 1
    for (int t = 0; t < NT - 2; t++) {
      if (t + 3 < NT) { ISSUE_X(t + 3, xslot); xslot = xslot == NS - 1 ? 0 : xslot + 1; WAIT_V(16); } else { WAIT_V(0); }
      BAR();
    }
  } else {
    QRegs q, qa; u32x2 sa, sb; unsigned tagv;
    Q_SC(sa, k1); Q_SC(sb, k2); TAG_LOAD(tagv);
    BAR();
    AWAIT(0xf000u, 0x2000u);
    Q_ROWS(qa, Q_SO(k1));
    ROUND(qa, sa, SB + XB + kc0 * KCB, 0, 1);
    AWAIT(0xf0u, 32u);
    Q_ROWS(q, Q_SO(k2));
    asm volatile("" :: "v"(ct));
    WAIT_L(0);
    asm volatile("" : "+v"(tagv));
    const unsigned tag = TAG_OF(tagv);
    unsigned fv = tagv;
    const unsigned fvo6 = wid == 6 ? fvo : 0u;
    BAR();
    asm volatile("global_load_dword %0, %1, %2 sc1" : "+v"(fv) : "v"(fvo6), "s"(flags) : "memory");
    ROUND(q, sb, STG_OFF + kc0 * KCB, 1, 1);
    WAIT_L(0);
   if (wid == 6) {
    const char* wbase = ws + (size_t)tn * NT * WB;
    const unsigned wv = (unsigned)(lane * 16), wv2 = wv + 4096, wv3 = wv + 8192, wv4 = wv + 12288;
    const unsigned ldsw = (unsigned)(size_t)(shm) + XB;
#define ISSUE_W(s, slot) do { const char* b_ = wbase + (size_t)KS(s) * WB; const unsigned l_ = ldsw + (slot) * SB;                \
      asm volatile("s_mov_b32 m0, %4\n\ts_nop 0\n\t"                                                                              \
                   "global_load_lds_dwordx4 %0, %5 sc1\n\tglobal_load_lds_dwordx4 %0, %5 offset:1024 sc1\n\t"                     \
                   "global_load_lds_dwordx4 %0, %5 offset:2048 sc1\n\tglobal_load_lds_dwordx4 %0, %5 offset:3072 sc1\n\t"         \
                   "s_add_u32 m0, m0, 0x1000\n\ts_nop 0\n\t"                                                                      \
                   "global_load_lds_dwordx4 %1, %5 sc1\n\tglobal_load_lds_dwordx4 %1, %5 offset:1024 sc1\n\t"                     \
                   "global_load_lds_dwordx4 %1, %5 offset:2048 sc1\n\tglobal_load_lds_dwordx4 %1, %5 offset:3072 sc1\n\t"         \
                   "s_add_u32 m0, m0, 0x1000\n\ts_nop 0\n\t"                                                                      \
                   "global_load_lds_dwordx4 %2, %5 sc1\n\tglobal_load_lds_dwordx4 %2, %5 offset:1024 sc1\n\t"                     \
                   "global_load_lds_dwordx4 %2, %5 offset:2048 sc1\n\tglobal_load_lds_dwordx4 %2, %5 offset:3072 sc1\n\t"         \
                   "s_add_u32 m0, m0, 0x1000\n\ts_nop 0\n\t"                                                                      \
                   "global_load_lds_dwordx4 %3, %5 sc1\n\tglobal_load_lds_dwordx4 %3, %5 offset:1024 sc1"                         \
                   :: "v"(wv), "v"(wv2), "v"(wv3), "v"(wv4), "s"(l_), "s"(b_) : "memory", "scc"); } while (0)
#define SET_RDY(f_) do { const unsigned long long b_ = __ballot((f_) != tag);                                                     \
      rdy = 2 + ((int)__builtin_ctzll(b_ | (1ull << 52)) >> 2); } while (0)
#define POLL_BLOCK() do { unsigned f_; asm volatile("global_load_dword %0, %1, %2 sc1\n\ts_waitcnt vmcnt(0)" : "=&v"(f_) : "v"(fvo), "s"(flags) : "memory"); \
      SET_RDY(f_); } while (0)
#define NEXT_W() do { nw++; wslot = wslot == NS - 1 ? 0 : wslot + 1; } while (0)
    int rdy = 2, nw = 3, wslot = 0; unsigned spins = 0;
    bool ppoll = true, preg = false;
#pragma unroll 1
    for (int t = 0; t < NT - 2; t++) {
      const int lim = t + 3 < NT ? t + 3 : NT - 1;
      if (preg) { WAIT_V(14); } else { WAIT_V(0); }
      asm volatile("" : "+v"(fv));
      if (ppoll) SET_RDY(fv);
      if (nw <= t + 2) {
#pragma unroll 1
        while (nw > rdy) { if (++spins > SPIN_MAX) { rdy = NT; break; } POLL_BLOCK(); }
        ISSUE_W(nw, wslot); NEXT_W();
      }
      ppoll = rdy < NT - 1 && rdy < t + 5;
      asm volatile("global_load_dword %0, %1, %2 sc1" : "+v"(fv) : "v"(ppoll ? fvo : zero), "s"(ppoll ? (const char*)flags : (const char*)am) : "memory");
      preg = nw <= lim && nw <= rdy;
      if (preg) { ISSUE_W(nw, wslot); NEXT_W(); WAIT_V(15); } else { WAIT_V(1); }
      BAR();
    }
    WAIT_V(0);
    asm volatile("" :: "v"(fv));
    if (tm == 0 && lane == 0) __hip_atomic_store(flags, tag, RLX_AGENT);
   } else {
#pragma unroll 1
    for (int t = 1; t < NT - 1; t++) {
      BAR();
      if (t == 2) {
#pragma unroll 1
        for (int j = 0; j < 4; j++) PUBLISH(STG_OFF + 2 * j * KCB, k2, j);
      }
      if (t == 3) {
        WAIT_V(0);
        if (lane < 4) __hip_atomic_store(flags + 256 + tm * 32 + 4 + lane, tag, RLX_AGENT);
      }
    }
    WAIT_V(0);
    asm volatile("" :: "v"(fv));
   }
  }
    unsigned fp = (unsigned)(size_t)shm_raw + FLG + (wid & 3) * 320 + 256;
#define UNIT(a0_, o0_, a1_, o1_) u32x4 p0_, p1_; do { unsigned f_;                                                                 \
      do { asm volatile("ds_read_b32 %0, %3\n\tds_read_b128 %1, %4 offset:%6\n\tds_read_b128 %2, %5 offset:%7\n\ts_waitcnt lgkmcnt(0)" \
                        : "=&v"(f_), "=&v"(p0_), "=&v"(p1_) : "v"(fp), "v"(a0_), "v"(a1_), "n"(o0_), "n"(o1_) : "memory");        \
      } while (__builtin_amdgcn_readfirstlane(f_) == 0); fp += 4; } while (0)
#define UNIT1(a0_, o0_) u32x4 p0_; do { unsigned f_;                                                                              \
      do { asm volatile("ds_read_b32 %0, %2\n\tds_read_b128 %1, %3 offset:%4\n\ts_waitcnt lgkmcnt(0)"                             \
                        : "=&v"(f_), "=&v"(p0_) : "v"(fp), "v"(a0_), "n"(o0_) : "memory");                                        \
      } while (__builtin_amdgcn_readfirstlane(f_) == 0); fp += 4; } while (0)
    O_ADDR(vw, rw, 0); O_ADDR(vs, rs, 1);
    { unsigned f_; do { asm volatile("ds_read_b32 %0, %1\n\ts_waitcnt lgkmcnt(0)" : "=v"(f_) : "v"(fp) : "memory"); } while (__builtin_amdgcn_readfirstlane(f_) == 0); }
    { UNIT(rw, 0, rw, 1024); O_ST(p0_, vw, 0, AUX_NT); O_ST(p1_, vw, 8, AUX_NT); }
    { UNIT(rw, 2048, rw, 3072); O_ST(p0_, vw, 16, AUX_NT); O_ST(p1_, vw, 24, AUX_NT); }
    { UNIT(rw, 4096, rw, 5120); O_ST(p0_, vw, 32, AUX_NT); O_ST(p1_, vw, 40, AUX_NT); }
    { UNIT(rw, 6144, rw, 7168); O_ST(p0_, vw, 48, AUX_NT); O_ST(p1_, vw, 56, AUX_NT); }
    { UNIT1(rs, 0); O_ST(p0_, vs, 0, AUX_NT); }
    { UNIT1(rs, 2048); O_ST(p0_, vs, 16, AUX_NT); }
    { UNIT1(rs, 4096); O_ST(p0_, vs, 32, AUX_NT); }
    { UNIT1(rs, 6144); O_ST(p0_, vs, 48, AUX_NT); }
  }
  asm volatile("" ::: "memory");
}

extern "C" void kernel_launch(void* const* d_in, const int* in_sizes, int n_in, void* d_out, int out_size, void* d_ws,
                              size_t ws_size, hipStream_t stream) {
  nf4_gemm<<<256, 512, 0, stream>>>((const uint16_t*)d_in[0], (const int*)d_in[1], (const uint16_t*)d_in[2],
                                    (const uint16_t*)d_in[3], (char*)d_ws, (uint16_t*)d_out);
}
```

```cpp
#include <hip/hip_runtime.h>
#include <hip/hip_bf16.h>
#include <stdint.h>

typedef __attribute__((ext_vector_type(8))) short bf16x8;
typedef __attribute__((ext_vector_type(4))) float f32x4;
typedef __attribute__((ext_vector_type(4))) int i32x4;
typedef __attribute__((ext_vector_type(4))) unsigned u32x4;
typedef __attribute__((ext_vector_type(2))) unsigned u32x2;
typedef __attribute__((ext_vector_type(2))) float f32x2;
typedef __attribute__((ext_vector_type(2))) __bf16 bf16x2_t;
typedef __attribute__((address_space(1))) unsigned gu32;

constexpr int M_ = 2048, K_ = 1024, N_ = 3584;
constexpr int BM = 256, BN = 112, BK = 64, NT = K_ / BK;
constexpr int XB = BM * BK * 2;
constexpr int WB = BN * BK * 2;
constexpr int KCB = BN * 16;
constexpr int SB = XB + WB;
constexpr int NS = 3;
constexpr int HDR = 4096;
constexpr int LUT_OFF = -HDR;
constexpr int FLG = 2560;
constexpr int CNT_OFF = -HDR + 2304;
constexpr int STG_OFF = NS * SB;
constexpr int SHM_BYTES = HDR + NS * SB + WB;
constexpr size_t WIMG_BYTES = (size_t)32 * NT * WB;
constexpr size_t SYNC_OFF = (size_t)8 << 20;
constexpr unsigned SPIN_MAX = 1u << 17;
constexpr unsigned CODE_TOUCH_MAX = 15468;
constexpr int AUX_NT = 2, AUX_SC1 = 16;

#define WAIT_V(n) asm volatile("s_waitcnt vmcnt(%0)" ::"n"(n) : "memory")
#define WAIT_L(n) asm volatile("s_waitcnt lgkmcnt(%0)" ::"n"(n) : "memory")
#define BAR() do { asm volatile("" ::: "memory"); __builtin_amdgcn_s_barrier(); asm volatile("" ::: "memory"); } while (0)
#define SCHED() __builtin_amdgcn_sched_barrier(0)
#define SGB(m_, n_) __builtin_amdgcn_sched_group_barrier(m_, n_, 0)
#define LGKM0() do { SCHED(); __builtin_amdgcn_s_waitcnt(0xC07F); SCHED(); } while (0)
#define TAG_OF(e) ((e) * 0x9E3779B1u + 0x7F4A7C15u)
#define RLX_AGENT __ATOMIC_RELAXED, __HIP_MEMORY_SCOPE_AGENT

__device__ __forceinline__ unsigned pack_bf16(float a, float b) {
  f32x2 v = {a, b};
  bf16x2_t r = __builtin_convertvector(v, bf16x2_t);
  return __builtin_bit_cast(unsigned, r);
}
__device__ __forceinline__ unsigned long long order_of(int tm, int tn) {
  const int t = tn & 15;
  const unsigned long long a = ((0x765432765432ull >> (8 * (t % 3))) & 0xffffff) ^ ((t & 1) * 0x111111);
  const unsigned long long b = (0xFEDCBA9FEDCBA9ull >> (4 * (t % 7))) & 0xfffffff;
  return (0x810 | a << 12 | b << 36) ^ (tm * 0x1111111111111111ull);
}

struct QRegs { i32x4 r[8]; };
__device__ __forceinline__ float smul(float a, float b) { float r; asm("v_mul_f32 %0, %1, %2" : "=v"(r) : "v"(a), "v"(b)); return r; }

__global__ __launch_bounds__(512, 2) void nf4_gemm(const uint16_t* __restrict__ x, const int* __restrict__ qw,
                                                   const uint16_t* __restrict__ cb, const uint16_t* __restrict__ am,
                                                   char* __restrict__ ws, uint16_t* __restrict__ out) {
  __shared__ __attribute__((aligned(1024))) char shm_raw[SHM_BYTES];
  char* const shm = shm_raw + HDR;
  unsigned long long pc0; asm volatile("s_getpc_b64 %0" : "=s"(pc0));
  unsigned ct;
  asm volatile("global_load_dword %0, %1, %2" : "=&v"(ct) : "v"(min((unsigned)((threadIdx.x & 15) * 1024 + ((blockIdx.x >> 3) & 7) * 128), CODE_TOUCH_MAX)), "s"(pc0) : "memory");
  const int bid = blockIdx.x;
  const int xcd = bid & 7, bi = bid >> 3;
  const int tn = (xcd >> 2) * 16 + (bi & 15), tm = (xcd & 3) * 2 + ((bi >> 4) & 1);
  const int k1 = tm ^ 1, k2 = tm + 8;
  const int tid = threadIdx.x, lane = tid & 63;
  const int wid = __builtin_amdgcn_readfirstlane(tid >> 6);
  const bool qact = (lane & 31) < 28;
  const int nc = qact ? (lane & 31) : 27;
  const int kc0 = 2 * (wid & 3);
  const int tj = tn & 3, cb0 = (tn >> 2) * 448;
  const int cw = tj * 64, cs = 256 + tj * 32, cq = 384 + tj * 16;
  const int col = nc * 4 + (nc < 16 ? cw : nc < 24 ? cs - 64 : cq - 96);
  const unsigned qvoff = (unsigned)(((kc0 + (lane >> 5)) * 8 * N_ + col) * 4);
  const unsigned avoff = (unsigned)(col * 2);
  const unsigned qs0 = (unsigned)(cb0 * 4);
  const unsigned xtv = (unsigned)((lane & 3) * (K_ * 2) + (((0x810 >> (4 * min(lane >> 2, 2))) & 15) ^ tm) * 128 + (tm * BM + (tn & 15) * 16 + (wid & 3) * 4) * (K_ * 2));
  asm volatile("" :: "v"(qvoff), "v"(avoff), "v"(xtv));
  asm volatile("" :: "s"(qs0 + (unsigned)tm * (BK * N_ * 4)));
  SCHED();
  asm volatile("" :: "s"(x), "s"(qw), "s"(cb), "s"(am), "s"(ws), "s"(out));
  gu32* flags = (gu32*)(ws + SYNC_OFF + (size_t)tn * 2048);
  typedef __attribute__((address_space(3))) unsigned lu32;
  lu32* cnt = (lu32*)(shm + CNT_OFF);
#define SIGNAL(v_) asm volatile("s_mov_b64 exec, 1\n\tds_add_u32 %0, %1\n\ts_mov_b64 exec, -1" :: "v"(cnt), "v"(v_) : "memory")
#define AWAIT(m_, n_) do { while ((__hip_atomic_load(cnt, __ATOMIC_RELAXED, __HIP_MEMORY_SCOPE_WORKGROUP) & (m_)) < (n_)) {}     \
      asm volatile("" ::: "memory"); SCHED(); } while (0)
  char* lut = shm + LUT_OFF;
  const __amdgpu_buffer_rsrc_t rsW = __builtin_amdgcn_make_buffer_rsrc((void*)ws, (short)0, (int)WIMG_BYTES, 0x00020000);
  const __amdgpu_buffer_rsrc_t rsQ = __builtin_amdgcn_make_buffer_rsrc((void*)qw, (short)0, K_ * N_ * 4, 0x00020000);
  const uint16_t* am0 = am + cb0;
  const unsigned zero = 0;
#define Q_ROW(Q, j, so_) asm volatile("buffer_load_dwordx4 %0, %1, %2, %3 offen" : "=&v"(Q.r[j]) : "v"(qvoff), "s"(rsQ), "s"((so_) + (j) * (N_ * 4)) : "memory")
#define Q_ROWS(Q, so_) do { _Pragma("unroll") for (int j_ = 0; j_ < 8; j_++) Q_ROW(Q, j_, so_); } while (0)
#define Q_SC(S, ks) asm volatile("global_load_dwordx2 %0, %1, %2" : "=&v"(S) : "v"(avoff), "s"(am0 + (ks) * N_) : "memory")
#define Q_SO(ks) (qs0 + (unsigned)(ks) * (BK * N_ * 4))
#define TAG_LOAD(T) asm volatile("global_load_dword %0, %1, %2 sc1" : "=&v"(T) : "v"(zero), "s"(flags) : "memory")
#define SCALES(S) do { asm volatile("" : "+v"(S));                                                                                \
      sc[0] = __builtin_bit_cast(float, S.x << 16); sc[1] = __builtin_bit_cast(float, S.x & 0xffff0000u);                         \
      sc[2] = __builtin_bit_cast(float, S.y << 16); sc[3] = __builtin_bit_cast(float, S.y & 0xffff0000u); } while (0)
#define R_LOOK(Q, a_, n_) do { WAIT_V(n_); asm volatile("" : "+v"(Q.r[2 * (a_)]), "+v"(Q.r[2 * (a_) + 1]));                        \
      _Pragma("unroll") for (int i_ = 0; i_ < 4; i_ += 2) { u32x2 d_;                                                             \
        asm("v_lshl_add_u64 %0, %1, 4, %2\n\tv_lshl_add_u64 %0, %0, 3, 0" : "=&v"(d_)                                            \
            : "v"((u32x2){(unsigned)Q.r[2 * (a_) + 1][i_], (unsigned)Q.r[2 * (a_) + 1][i_ + 1]}),                                 \
              "v"((u32x2){(unsigned)Q.r[2 * (a_)][i_], (unsigned)Q.r[2 * (a_)][i_ + 1]}));                                        \
        lv[i_][a_] = *(const f32x2*)(lut + d_.x); lv[i_ + 1][a_] = *(const f32x2*)(lut + d_.y); }                                 \
      SCHED(); } while (0)
#define R_PACK(a_, sm) do { _Pragma("unroll") for (int i_ = 0; i_ < 4; i_++)                                                      \
      po[i_][a_] = (sm) ? pack_bf16(smul(lv[i_][a_].x, sc[i_]), smul(lv[i_][a_].y, sc[i_])) : pack_bf16(lv[i_][a_].x * sc[i_], lv[i_][a_].y * sc[i_]); \
      SCHED(); } while (0)
#define R_WRITE(dst) do { _Pragma("unroll") for (int i_ = 0; i_ < 4; i_++) *(u32x4*)(shm + (dst) + (i_ < 2 ? io0 : io1) + i_ * 896) = po[i_]; SCHED(); } while (0)
#define ROUND(Q, S, dst, e, sm) do { R_LOOK(Q, 0, (e) + 6); SCALES(S); R_LOOK(Q, 1, (e) + 4); R_PACK(0, sm);                      \
      R_LOOK(Q, 2, (e) + 2); R_PACK(1, sm); R_LOOK(Q, 3, e); R_PACK(2, sm); R_PACK(3, sm); R_WRITE(dst); } while (0)
#define PUBLISH(src, ks, kcp) do { WAIT_L(0); if (lane < 56) {                                                                    \
        const unsigned g_ = (unsigned)((tn * NT + (ks)) * WB + (kcp) * (2 * KCB) + lane * 16); u32x4 pv_[4];                      \
        _Pragma("unroll") for (int i_ = 0; i_ < 4; i_++) pv_[i_] = *(const u32x4*)(shm + (src) + lane * 16 + i_ * 896);           \
        _Pragma("unroll") for (int i_ = 0; i_ < 4; i_++) __builtin_amdgcn_raw_buffer_store_b128(pv_[i_], rsW, g_ + i_ * 896, 0, AUX_SC1); } \
      SCHED(); } while (0)
  f32x2 lv[4][4]; float sc[4]; u32x4 po[4];
  const __amdgpu_buffer_rsrc_t rsO = __builtin_amdgcn_make_buffer_rsrc((void*)out, (short)0, M_ * N_ * 2, 0x00020000);
#define O_ADDR(vo, ro, k) unsigned vo; int ro; do { const int r_ = lane >> (3 - (k)), c_ = lane & (7 >> (k));                     \
      vo = (unsigned)(((tm * BM + (wid & 3) * 64 + r_) * N_ + cb0 + ((k) == 0 ? cw : (k) == 1 ? cs : cq)) * 2 + c_ * 16);     \
      ro = HDR + ((k) ? 0 : SB) + (wid & 3) * 8192 + r_ * 128 + (((c_ + ((k) & 2) * 2) ^ (r_ & 7)) << 4);                         \
      asm volatile("" : "+v"(vo), "+v"(ro)); } while (0)
#define O_RD(ro, r_) (*(const u32x4*)(shm_raw + (ro) + (r_) * 128))
#define O_ST(v_, vo, r_, aux) __builtin_amdgcn_raw_buffer_store_b128(v_, rsO, (vo) + (r_) * (N_ * 2), 0, aux)
#define IO_OF() const int io0 = nc * 16 + (lane >> 5) * 448, io1 = nc * 16 + 448 - (lane >> 5) * 448

  if (__builtin_expect(wid < 4, 1)) {
    QRegs q; u32x2 qsv; unsigned c0, c1, tagv, xt0;
    asm volatile("global_load_ushort %0, %2, %4\n\tglobal_load_ushort %1, %3, %4" : "=&v"(c0), "=&v"(c1)
                 : "v"((unsigned)((lane & 15) * 2)), "v"((unsigned)((wid * 4 + (lane >> 4)) * 2)), "s"(cb) : "memory");
    TAG_LOAD(tagv);
    Q_SC(qsv, tm);
    asm volatile("global_load_dword %0, %1, %2" : "=&v"(xt0) : "v"(xtv), "s"(x) : "memory");
    Q_ROWS(q, Q_SO(tm));
    SCHED();
    IO_OF();
    const unsigned gb = (unsigned)((tn * NT + tm) * WB + kc0 * KCB);
    unsigned go0 = qact ? gb + io0 : 0x7fffffffu, go1 = qact ? gb + io1 : 0x7fffffffu;
    asm volatile("" : "+v"(go0), "+v"(go1));
    f32x4 acc[4][7];
#pragma unroll
    for (int m = 0; m < 4; m++)
#pragma unroll
      for (int n = 0; n < 7; n++) { acc[m][n] = (f32x4){0.f, 0.f, 0.f, 0.f}; asm volatile("" : "+v"(acc[m][n])); }
    WAIT_V(11);
    asm volatile("" : "+v"(c0), "+v"(c1)); asm volatile("" :: "v"(ct));
    *cnt = 0;
    *(u32x2*)(lut + tid * 8) = (u32x2){c0 << 16, c1 << 16};
    int fa = FLG + wid * 320 + lane * 4;
    asm volatile("" : "+v"(fa));
    *(unsigned*)(shm_raw + fa + 64) = 0u;
    WAIT_L(0);
    BAR();
    ROUND(q, qsv, XB + kc0 * KCB, 0, 0);
    WAIT_L(0);
    SIGNAL(1u);
    SCHED();
    __builtin_amdgcn_raw_buffer_store_b128(po[0], rsW, go0, 0, AUX_SC1);
    __builtin_amdgcn_raw_buffer_store_b128(po[1], rsW, go0 + 896, 0, AUX_SC1);
    __builtin_amdgcn_raw_buffer_store_b128(po[2], rsW, go1 + 2 * 896, 0, AUX_SC1);
    __builtin_amdgcn_raw_buffer_store_b128(po[3], rsW, go1 + 3 * 896, 0, AUX_SC1);
    SCHED();
    const int fr = lane & 15, fq = lane >> 4;
    const int xo0 = (wid * 64 + fr) * 128 + ((fq ^ (fr >> 1)) << 4), xo1 = xo0 ^ 64;
    const int wo = XB + (fq >> 1) * (2 * KCB) + (fr & 3) * 896 + ((fq ^ (fr >> 1)) & 1) * 448 + (fr >> 2) * 16;
    bf16x8 xa[4], wa[7], xb[4], wb[7];
#define RD(xf, wf, aw_, ax_, hf) do {                                                                 \
      _Pragma("unroll") for (int n_ = 0; n_ < 7; n_++) wf[n_] = *(const bf16x8*)(shm + (aw_) + (hf) * (4 * KCB) + n_ * 64); \
      _Pragma("unroll") for (int m_ = 0; m_ < 4; m_++) xf[m_] = *(const bf16x8*)(shm + (ax_) + m_ * 2048); } while (0)
#define MMR(xf, wf, i0, i1) do { _Pragma("unroll") for (int i_ = (i0); i_ < (i1); i_++) {             \
      const int m_ = i_ / 7, n_ = m_ & 1 ? 6 - i_ % 7 : i_ % 7;                                       \
      acc[m_][n_] = __builtin_amdgcn_mfma_f32_16x16x32_bf16(wf[n_], xf[m_], acc[m_][n_], 0, 0, 0); } } while (0)
    O_ADDR(vq, rq, 2);
    int e0 = HDR + wid * 8192 + fr * 128 + (((fq >> 1) ^ (fr & 7)) << 4) + (fq & 1) * 8, e1 = e0 ^ 32, e2 = e0 ^ 64, e3 = e0 ^ 96;
    asm volatile("" : "+v"(e0), "+v"(e1), "+v"(e2), "+v"(e3));
    AWAIT(15u, 6u);
#define INTER11() do { _Pragma("unroll") for (int i_ = 0; i_ < 11; i_++) { SGB(0x8, 2); SGB(0x100, 1); } } while (0)
#define STAGE(st) do { int aw_ = wo + (st) * SB; asm volatile("" : "+v"(aw_));                                                    \
      RD(xa, wa, aw_, xo0 + (st) * SB, 0);                                                                                        \
      SCHED();                                                                                                                    \
      RD(xb, wb, aw_, xo1 + (st) * SB, 1);                                                                                        \
      MMR(xa, wa, 0, 28);                                                                                                         \
      INTER11(); SGB(0x8, 6);                                                                                                     \
      SCHED();                                                                                                                    \
      if ((st) == 0) {                                                          \
        WAIT_V(0); asm volatile("" : "+v"(tagv) : "v"(xt0));                                                                      \
        if (lane == 0) __hip_atomic_store(flags + 256 + tm * 32 + wid, TAG_OF(tagv), RLX_AGENT); }                                \
      MMR(xb, wb, 0, 14);                                                                                                         \
      SCHED();                                                                                                                    \
      MMR(xb, wb, 14, 28);                                                                                                        \
      SCHED(); } while (0)
    STAGE(0);
    BAR();
    STAGE(1);
    asm volatile(".rept 1\n\ts_nop 0\n\t.endr");
    BAR();
    int aw = wo + STG_OFF - XB, a0 = xo0 + 2 * SB, a1 = xo1 + 2 * SB, so = 2 * SB;
    RD(xa, wa, aw, a0, 0);
    asm volatile("" : "+v"(aw), "+v"(a1));
#define HALF_A() do { SCHED();                                                                                                    \
      RD(xb, wb, aw, a1, 1);                                                                                                      \
      MMR(xa, wa, 0, 27);                                                                                                         \
      so = so == 2 * SB ? 0 : so + SB;                                                                                            \
      aw = wo + so; a0 = xo0 + so; a1 = xo1 + so;                                                                                 \
      INTER11(); SGB(0x8, 1); SGB(0x4, 3); SGB(0x8, 1); SGB(0x2, 2); SGB(0x8, 1); SGB(0x2, 1); SGB(0x8, 2);                       \
      asm volatile("" : "+v"(aw), "+v"(a0), "+v"(a1));                                                                            \
      LGKM0();                                                                                                                    \
      MMR(xa, wa, 27, 28);                                                                                                        \
      SCHED();                                                                                                                    \
      BAR();                                                                                                             \
      SCHED(); } while (0)
#pragma unroll 1
    for (int t = 2; t < NT - 3; t++) {
      HALF_A();
      RD(xa, wa, aw, a0, 0);
      MMR(xb, wb, 0, 28);
      INTER11(); SGB(0x8, 4); SGB(0x4, 2); SGB(0x8, 2);
    }
    HALF_A();
    SCHED();
#define LD(o_) (*(const bf16x8*)(shm + (o_)))
#define MF(m_, n_, xv, wf) do { asm volatile("v_mfma_f32_16x16x32_bf16 %0, %1, %2, %0" : "+v"(acc[m_][n_]) : "v"(wf[n_]), "v"(xv)); SCHED(); } while (0)
#define RX(xf, b_, m_) xf[m_] = LD((b_) + (m_) * 2048)
#define RW(wf, b_, hf, n_) wf[n_] = LD((b_) + (hf) * (4 * KCB) + (n_) * 64)
#define CV(m_, j_) pk[m_][j_] = pack_bf16(acc[m_][(j_) / 2][((j_) & 1) * 2], acc[m_][(j_) / 2][((j_) & 1) * 2 + 1])
#define WL(n_) do { SCHED(); __builtin_amdgcn_s_waitcnt(0xC07F | ((n_) << 8)); SCHED(); } while (0)
#define W64(m_, n_) *(u32x2*)(shm_raw + (((n_) & 3) == 0 ? e0 : ((n_) & 3) == 1 ? e1 : ((n_) & 3) == 2 ? e2 : e3) + ((n_) < 4 ? SB : 0) + (m_) * 2048) = (u32x2){pk[m_][2 * (n_)], pk[m_][2 * (n_) + 1]}
#define W32(m_, n_, h_) *(unsigned*)(shm_raw + (((n_) & 3) == 0 ? e0 : ((n_) & 3) == 1 ? e1 : ((n_) & 3) == 2 ? e2 : e3) + ((n_) < 4 ? SB : 0) + (m_) * 2048 + 4 * (h_)) = pk[m_][2 * (n_) + (h_)]
#define WA(m_, n_) W32(m_, n_, 0)
#define WB_(m_, n_) W32(m_, n_, 1)
#define FLAG(u_) do { asm volatile("" ::: "memory"); *(int*)(shm_raw + fa + 4 * (u_)) = fa; asm volatile("" ::: "memory"); } while (0)
    {
      bf16x8 xc[4], xd[4], xe[4], wc[7], wd[7], we[7];
      unsigned pk[4][14];
      u32x4 ov[2];
      MF(0, 0, xb[0], wb); RX(xa, a0, 0); SCHED();
      MF(0, 1, xb[0], wb); RW(wa, aw, 0, 0); SCHED();
      MF(0, 2, xb[0], wb); RW(wa, aw, 0, 1); SCHED();
      MF(0, 3, xb[0], wb); RW(wa, aw, 0, 2); SCHED();
      MF(1, 0, xb[1], wb); RW(wa, aw, 0, 3); SCHED();
      MF(1, 1, xb[1], wb); RX(xc, a1, 0); SCHED();
      MF(1, 2, xb[1], wb); RW(wc, aw, 1, 0); SCHED();
      MF(1, 3, xb[1], wb); RW(wc, aw, 1, 1); SCHED();
      MF(2, 0, xb[2], wb); RW(wc, aw, 1, 2); SCHED();
      MF(2, 1, xb[2], wb); RW(wc, aw, 1, 3); SCHED();
      MF(2, 2, xb[2], wb); RX(xa, a0, 1); SCHED();
      MF(2, 3, xb[2], wb); RX(xc, a1, 1); SCHED();
      MF(3, 0, xb[3], wb);
      MF(3, 1, xb[3], wb); RX(xa, a0, 2); SCHED();
      BAR();
      SCHED();
      MF(3, 2, xb[3], wb); RX(xd, xo0, 0); SCHED();
      MF(3, 3, xb[3], wb); RW(wd, aw + WB, 0, 0); WL(4); SCHED();
      MF(0, 0, xa[0], wa); RX(xe, xo1, 0); SCHED();
      MF(0, 1, xa[0], wa); RW(we, aw + WB, 1, 0); SCHED();
      MF(0, 2, xa[0], wa); RW(wd, aw + WB, 0, 1); SCHED();
      MF(0, 3, xa[0], wa); RW(we, aw + WB, 1, 1); SCHED();
      MF(0, 0, xc[0], wc); RW(wd, aw + WB, 0, 2); RW(we, aw + WB, 1, 2); SCHED();
      MF(0, 1, xc[0], wc); RW(wd, aw + WB, 0, 3); RW(we, aw + WB, 1, 3); SCHED();
      MF(0, 2, xc[0], wc); RX(xd, xo0, 1); RX(xe, xo1, 1); SCHED();
      MF(0, 3, xc[0], wc); RX(xc, a1, 2); WL(8); SCHED();
      MF(0, 0, xd[0], wd); RX(xd, xo0, 2); SCHED();
      MF(0, 0, xe[0], we);
      MF(0, 1, xd[0], wd); WL(4); SCHED();
      MF(0, 1, xe[0], we); WL(2); SCHED();
      MF(0, 2, xd[0], wd); CV(0, 0); CV(0, 1); SCHED();
      MF(0, 2, xe[0], we); W64(0, 0); SCHED();
      MF(0, 3, xd[0], wd); CV(0, 2); CV(0, 3); SCHED();
      MF(0, 3, xe[0], we); W64(0, 1); SCHED();
      MF(1, 0, xa[1], wa); CV(0, 4); CV(0, 5); SCHED();
      MF(1, 0, xc[1], wc); W64(0, 2); SCHED();
      MF(1, 0, xd[1], wd); CV(0, 6); CV(0, 7); SCHED();
      MF(1, 0, xe[1], we); W64(0, 3); SCHED();
      MF(1, 1, xa[1], wa); FLAG(1); SCHED();
      MF(1, 1, xc[1], wc); RX(xe, xo1, 2); RX(xa, a0, 3); SCHED();
      MF(1, 1, xd[1], wd); CV(1, 0); CV(1, 1); SCHED();
      MF(1, 1, xe[1], we); W64(1, 0); SCHED();
      MF(1, 2, xa[1], wa); RX(xc, a1, 3); RX(xd, xo0, 3); SCHED();
      MF(1, 2, xc[1], wc); RX(xe, xo1, 3); SCHED();
      MF(1, 2, xd[1], wd); CV(1, 2); CV(1, 3); SCHED();
      MF(1, 2, xe[1], we); W64(1, 1); SCHED();
      MF(1, 3, xa[1], wa); RW(wb, wo + SB, 1, 4); SCHED();
      MF(1, 3, xc[1], wc);
      MF(1, 3, xd[1], wd); CV(1, 4); CV(1, 5); SCHED();
      MF(1, 3, xe[1], we); W64(1, 2); SCHED();
      MF(2, 0, xa[2], wa); RW(wa, aw, 0, 4); WL(4); SCHED();
      MF(2, 0, xc[2], wc);
      MF(2, 0, xd[2], wd); CV(1, 6); CV(1, 7); SCHED();
      MF(2, 0, xe[2], we); W64(1, 3); SCHED();
      MF(2, 1, xa[2], wa); FLAG(2); SCHED();
      MF(2, 1, xc[2], wc); RW(wc, aw, 1, 4); SCHED();
      MF(2, 1, xd[2], wd); CV(2, 0); CV(2, 1); SCHED();
      MF(2, 1, xe[2], we); W64(2, 0); SCHED();
      MF(2, 2, xa[2], wa); RW(wd, aw + WB, 0, 4); SCHED();
      MF(2, 2, xc[2], wc);
      MF(2, 2, xd[2], wd); CV(2, 2); CV(2, 3); SCHED();
      MF(2, 2, xe[2], we); W64(2, 1); SCHED();
      MF(2, 3, xa[2], wa); RW(we, aw + WB, 1, 4); SCHED();
      MF(2, 3, xc[2], wc);
      MF(2, 3, xd[2], wd); CV(2, 4); CV(2, 5); SCHED();
      MF(2, 3, xe[2], we); W64(2, 2); SCHED();
      MF(3, 0, xa[3], wa); RW(wb, wo + SB, 1, 5); RW(wa, aw, 0, 5); SCHED();
      MF(3, 0, xc[3], wc); RW(wc, aw, 1, 5); SCHED();
      MF(3, 0, xd[3], wd); CV(2, 6); CV(2, 7); SCHED();
      MF(3, 0, xe[3], we); W64(2, 3); SCHED();
      MF(3, 1, xa[3], wa); FLAG(3); SCHED();
      MF(3, 1, xc[3], wc); RW(wd, aw + WB, 0, 5); RW(we, aw + WB, 1, 5); SCHED();
      MF(3, 1, xd[3], wd); CV(3, 0); CV(3, 1); SCHED();
      MF(3, 1, xe[3], we); W64(3, 0); SCHED();
      MF(3, 2, xa[3], wa);
      MF(3, 2, xc[3], wc);
      MF(3, 2, xd[3], wd); CV(3, 2); CV(3, 3); SCHED();
      MF(3, 2, xe[3], we); W64(3, 1); SCHED();
      MF(3, 3, xa[3], wa);
      MF(3, 3, xc[3], wc); WL(2); SCHED();
      MF(3, 3, xd[3], wd); CV(3, 4); CV(3, 5); SCHED();
      MF(3, 3, xe[3], we); W64(3, 2); SCHED();
      MF(0, 4, xb[0], wb); RW(wb, wo + SB, 1, 6); SCHED();
      MF(0, 4, xa[0], wa); RW(wa, aw, 0, 6); SCHED();
      MF(0, 4, xc[0], wc); CV(3, 6); CV(3, 7); SCHED();
      MF(0, 4, xd[0], wd); W64(3, 3); SCHED();
      MF(0, 4, xe[0], we); FLAG(4); SCHED();
      MF(0, 5, xb[0], wb); RW(wc, aw, 1, 6); RW(wd, aw + WB, 0, 6); SCHED();
      MF(0, 5, xa[0], wa); RW(we, aw + WB, 1, 6); SCHED();
      MF(0, 5, xc[0], wc); CV(0, 8); CV(0, 9); SCHED();
      MF(0, 5, xd[0], wd); WA(0, 4); SCHED();
      MF(0, 5, xe[0], we); WB_(0, 4); SCHED();
      MF(1, 4, xb[1], wb);
      MF(1, 4, xa[1], wa);
      MF(1, 4, xc[1], wc); CV(0, 10); CV(0, 11); SCHED();
      MF(1, 4, xd[1], wd); WA(0, 5); SCHED();
      MF(1, 4, xe[1], we); WB_(0, 5); SCHED();
      MF(1, 5, xb[1], wb); FLAG(5); SCHED();
      MF(1, 5, xa[1], wa);
      MF(1, 5, xc[1], wc); CV(1, 8); CV(1, 9); SCHED();
      MF(1, 5, xd[1], wd); WA(1, 4); SCHED();
      MF(1, 5, xe[1], we); WB_(1, 4); SCHED();
      MF(2, 4, xb[2], wb);
      MF(2, 4, xa[2], wa);
      MF(2, 4, xc[2], wc); CV(1, 10); CV(1, 11); SCHED();
      MF(2, 4, xd[2], wd); WA(1, 5); SCHED();
      MF(2, 4, xe[2], we); WB_(1, 5); SCHED();
      MF(2, 5, xb[2], wb); FLAG(6); SCHED();
      MF(2, 5, xa[2], wa);
      MF(2, 5, xc[2], wc); CV(2, 8); CV(2, 9); SCHED();
      MF(2, 5, xd[2], wd); WA(2, 4); SCHED();
      MF(2, 5, xe[2], we); WB_(2, 4); SCHED();
      MF(3, 4, xb[3], wb);
      MF(3, 4, xa[3], wa);
      MF(3, 4, xc[3], wc); CV(2, 10); CV(2, 11); SCHED();
      MF(3, 4, xd[3], wd); WA(2, 5); SCHED();
      MF(3, 4, xe[3], we); WB_(2, 5); SCHED();
      MF(3, 5, xb[3], wb); FLAG(7); SCHED();
      MF(3, 5, xa[3], wa); WL(15); SCHED();
      MF(3, 5, xc[3], wc); CV(3, 8); CV(3, 9); SCHED();
      MF(3, 5, xd[3], wd); WA(3, 4); SCHED();
      MF(3, 5, xe[3], we); WB_(3, 4); SCHED();
      MF(0, 6, xb[0], wb);
      MF(0, 6, xa[0], wa);
      MF(0, 6, xc[0], wc); CV(3, 10); CV(3, 11); SCHED();
      MF(0, 6, xd[0], wd); WA(3, 5); SCHED();
      MF(0, 6, xe[0], we); WB_(3, 5); SCHED();
      MF(1, 6, xb[1], wb); FLAG(8); SCHED();
      MF(1, 6, xa[1], wa);
      MF(1, 6, xc[1], wc); CV(0, 12); CV(0, 13); SCHED();
      MF(1, 6, xd[1], wd); WA(0, 6); SCHED();
      MF(1, 6, xe[1], we); WB_(0, 6); SCHED();
      MF(2, 6, xb[2], wb);
      MF(2, 6, xa[2], wa);
      MF(2, 6, xc[2], wc); CV(1, 12); CV(1, 13); SCHED();
      MF(2, 6, xd[2], wd); WA(1, 6); SCHED();
      MF(2, 6, xe[2], we); WB_(1, 6); SCHED();
      MF(3, 6, xb[3], wb); ov[0] = O_RD(rq, 0); SCHED();
      MF(3, 6, xa[3], wa);
      MF(3, 6, xc[3], wc); CV(2, 12); CV(2, 13); SCHED();
      MF(3, 6, xd[3], wd); WA(2, 6); SCHED();
      MF(3, 6, xe[3], we); WB_(2, 6); SCHED();
      O_ST(ov[0], vq, 0, 0); SCHED();
      asm volatile("s_nop 6");
      SCHED();
      CV(3, 12); CV(3, 13); W64(3, 6); ov[1] = O_RD(rq, 32); SCHED();
      O_ST(ov[1], vq, 32, 0);
    }
  } else {
    IO_OF();
    const unsigned long long ord = order_of(tm, tn);
#define KS(s_) ((int)(ord >> (4 * (s_))) & 15)
    const int h = wid & 1;
    unsigned xv[16];
#pragma unroll
    for (int i = 0; i < 16; i++) xv[i] = (unsigned)((8 * i + (lane >> 3)) * (K_ * 2) + (((lane & 7) ^ ((i & 1) * 4 + (lane >> 4))) << 4));
    const char* xbase = (const char*)(x + (size_t)(tm * BM + h * 128) * K_);
    const unsigned ldsx = (unsigned)(size_t)(shm) + h * 16384;
#define X8(o, l_, b_)                                                                                                             \
      asm volatile("s_mov_b32 m0, %8\n\ts_nop 0\n\tglobal_load_lds_dwordx4 %0, %9\n\t"                                            \
                   "s_add_u32 m0, m0, 0x400\n\ts_nop 0\n\tglobal_load_lds_dwordx4 %1, %9\n\t"                                     \
                   "s_add_u32 m0, m0, 0x400\n\ts_nop 0\n\tglobal_load_lds_dwordx4 %2, %9\n\t"                                     \
                   "s_add_u32 m0, m0, 0x400\n\ts_nop 0\n\tglobal_load_lds_dwordx4 %3, %9\n\t"                                     \
                   "s_add_u32 m0, m0, 0x400\n\ts_nop 0\n\tglobal_load_lds_dwordx4 %4, %9\n\t"                                     \
                   "s_add_u32 m0, m0, 0x400\n\ts_nop 0\n\tglobal_load_lds_dwordx4 %5, %9\n\t"                                     \
                   "s_add_u32 m0, m0, 0x400\n\ts_nop 0\n\tglobal_load_lds_dwordx4 %6, %9\n\t"                                     \
                   "s_add_u32 m0, m0, 0x400\n\ts_nop 0\n\tglobal_load_lds_dwordx4 %7, %9"                                         \
                   :: "v"(xv[o]), "v"(xv[o + 1]), "v"(xv[o + 2]), "v"(xv[o + 3]), "v"(xv[o + 4]), "v"(xv[o + 5]), "v"(xv[o + 6]), "v"(xv[o + 7]), \
                      "s"(l_), "s"(b_) : "memory", "scc")
#define ISSUE_X(s, slot) do { const char* b_ = xbase + KS(s) * 128; const unsigned l_ = ldsx + (slot) * SB;                       \
      X8(0, l_, b_); X8(8, l_ + 8192, b_); } while (0)
    const int fj = (int)(ord >> (4 * min(lane >> 2, 12) + 12));
    const unsigned fvo = (unsigned)(1024 + (fj & 7) * 128 + (fj & 8) * 2 + (lane & 3) * 4);
    O_ADDR(vw, rw, 0); O_ADDR(vs, rs, 1);
    unsigned fp = (unsigned)(size_t)shm_raw + FLG + (wid & 3) * 320 + 256;
    asm volatile("" : "+v"(fp));
    unsigned va[12];
#pragma unroll
    for (int i = 0; i < 12; i++) { va[i] = i < 8 ? vw + i * 8 * (N_ * 2) : vs + (i - 8) * 16 * (N_ * 2); asm volatile("" : "+v"(va[i])); }
  if (wid < 6) {
    QRegs qa, q; u32x2 sa, sb;
    Q_SC(sa, k1);
    Q_SC(sb, k2);
    BAR();
    ISSUE_X(0, 0);
    SIGNAL(0x1000u);
    Q_ROWS(qa, Q_SO(k1));
    WAIT_V(8);
    SIGNAL(1u);
    { const char* b_ = xbase + k1 * 128; const unsigned l_ = ldsx + SB; X8(0, l_, b_); X8(8, l_ + 8192, b_); }
    SIGNAL(16u);
    asm volatile("" :: "v"(ct));
    Q_ROWS(q, Q_SO(k2));
    ROUND(qa, sa, SB + XB + kc0 * KCB, 24, 1);
    WAIT_V(8);
    WAIT_L(0);
    BAR();
    ROUND(q, sb, STG_OFF + kc0 * KCB, 0, 1);
    ISSUE_X(2, 2);
    WAIT_L(0);
    unsigned long long o = ord >> 12; unsigned l_ = ldsx;
#pragma unroll 1
    for (int t = 0; t < NT - 3; t++) {
      const char* b_ = xbase + ((unsigned)o & 15) * 128; unsigned l2 = l_ + 8192;
      asm volatile("" : "+s"(b_), "+s"(l_), "+s"(l2));
      if (t) { WAIT_V(16); BAR(); }
      X8(0, l_, b_); X8(8, l2, b_);
      o >>= 4; l_ = l_ == ldsx + 2 * SB ? ldsx : l_ + SB;
    }
    WAIT_V(16); BAR();
    WAIT_V(0); BAR();
  } else {
    QRegs q, qa; u32x2 sa, sb; unsigned tagv;
    Q_SC(sa, k1); Q_SC(sb, k2); TAG_LOAD(tagv);
    BAR();
    AWAIT(0xf000u, 0x2000u);
    Q_ROWS(qa, Q_SO(k1));
    ROUND(qa, sa, SB + XB + kc0 * KCB, 0, 1);
    AWAIT(0xf0u, 32u);
    Q_ROWS(q, Q_SO(k2));
    asm volatile("" :: "v"(ct));
    WAIT_L(0);
    asm volatile("" : "+v"(tagv));
    const unsigned tag = TAG_OF(tagv);
    unsigned fv = tagv;
    const unsigned fvo6 = wid == 6 ? fvo : 0u;
    BAR();
    asm volatile("global_load_dword %0, %1, %2 sc1" : "+v"(fv) : "v"(fvo6), "s"(flags) : "memory");
    ROUND(q, sb, STG_OFF + kc0 * KCB, 1, 1);
    WAIT_L(0);
    const char* wbase = ws + (size_t)tn * NT * WB;
    const unsigned wv = (unsigned)(lane * 16), wv2 = wv + 4096, wv3 = wv + 8192, wv4 = wv + 12288;
    const unsigned ldsw = (unsigned)(size_t)(shm) + XB;
#define W4(v_, l_, b_) asm volatile("s_mov_b32 m0, %1\n\ts_nop 0\n\t"                                                             \
                   "global_load_lds_dwordx4 %0, %2 sc1\n\tglobal_load_lds_dwordx4 %0, %2 offset:1024 sc1\n\t"                     \
                   "global_load_lds_dwordx4 %0, %2 offset:2048 sc1\n\tglobal_load_lds_dwordx4 %0, %2 offset:3072 sc1" :: "v"(v_), "s"(l_), "s"(b_) : "memory")
#define W2(v_, l_, b_) asm volatile("s_mov_b32 m0, %1\n\ts_nop 0\n\t"                                                             \
                   "global_load_lds_dwordx4 %0, %2 sc1\n\tglobal_load_lds_dwordx4 %0, %2 offset:1024 sc1" :: "v"(v_), "s"(l_), "s"(b_) : "memory")
#define W1(v_, o_, l_, b_) asm volatile("s_mov_b32 m0, %1\n\ts_nop 0\n\tglobal_load_lds_dwordx4 %0, %2 offset:%3 sc1" :: "v"(v_), "s"(l_), "s"(b_), "n"(o_) : "memory")
   if (__builtin_expect(wid == 6, 1)) {
    asm volatile(
      "s_lshr_b64 s[64:65], %[ord], 12\n\ts_mov_b32 s66, 0\n\ts_mov_b32 s67, 3\n\ts_mov_b32 s69, %[l0]\n\t"
      "s_mov_b32 s70, 0\n\ts_mov_b32 s71, 0\n\ts_mov_b32 m0, s69\n\ts_and_b32 s60, s64, 15\n\ts_mulk_i32 s60, 0x3800\n\t"
      "s_add_u32 s60, s60, %[blo]\n\ts_addc_u32 s61, %[bhi], 0\n\ts_waitcnt vmcnt(0)\n\t"
      "v_cmp_ne_u32_e64 s[62:63], %[fv], %[tag]\n\ts_bitset1_b32 s63, 20\n\ts_ff1_i32_b64 s68, s[62:63]\n\t"
      "s_lshr_b32 s68, s68, 2\n\ts_add_i32 s68, s68, 2\n\ts_mov_b64 vcc, 0\n\ts_cmp_gt_i32 s67, s68\n\t"
      "s_cbranch_scc1 .Lw%=\n\ts_branch .Lf%=\n\t"
      ".La%=:\n\ts_waitcnt vmcnt(14)\n\t"
      ".Lb%=:\n\ts_waitcnt vmcnt(15)\n\t"
      "s_barrier\n\ts_cbranch_scc1 .Ls%=\n\ts_sleep 5\n\t"
      ".Lf%=:\n\tglobal_load_lds_dwordx4 %[w0], s[60:61] sc1\n\t"
      "global_load_lds_dwordx4 %[w0], s[60:61] offset:1024 sc1\n\t"
      "global_load_lds_dwordx4 %[w0], s[60:61] offset:2048 sc1\n\t"
      "global_load_lds_dwordx4 %[w0], s[60:61] offset:3072 sc1\n\ts_cbranch_vccz .Lg%=\n\ts_waitcnt vmcnt(14)\n\t"
      "v_cmp_ne_u32_e64 s[62:63], %[fv], %[tag]\n\ts_bitset1_b32 s63, 20\n\ts_ff1_i32_b64 s68, s[62:63]\n\t"
      "s_lshr_b32 s68, s68, 2\n\ts_add_i32 s68, s68, 2\n\t"
      ".Lg%=:\n\ts_add_i32 s62, s66, 5\n\ts_min_i32 s62, s62, 15\n\ts_cmp_lt_i32 s68, s62\n\ts_cselect_b64 vcc, -1, 0\n\t"
      "s_cbranch_scc0 .Lh%=\n\tglobal_load_dword %[fv], %[fvo], %[fl] sc1\n\t"
      ".Lh%=:\n\ts_add_u32 m0, m0, 0x1000\n\ts_nop 0\n\tglobal_load_lds_dwordx4 %[w1], s[60:61] sc1\n\t"
      "global_load_lds_dwordx4 %[w1], s[60:61] offset:1024 sc1\n\t"
      "global_load_lds_dwordx4 %[w1], s[60:61] offset:2048 sc1\n\t"
      "global_load_lds_dwordx4 %[w1], s[60:61] offset:3072 sc1\n\ts_add_u32 m0, m0, 0x1000\n\ts_nop 0\n\t"
      "global_load_lds_dwordx4 %[w2], s[60:61] sc1\n\tglobal_load_lds_dwordx4 %[w2], s[60:61] offset:1024 sc1\n\t"
      "global_load_lds_dwordx4 %[w2], s[60:61] offset:2048 sc1\n\t"
      "global_load_lds_dwordx4 %[w2], s[60:61] offset:3072 sc1\n\ts_add_u32 m0, m0, 0x1000\n\ts_nop 0\n\t"
      "global_load_lds_dwordx4 %[w3], s[60:61] sc1\n\tglobal_load_lds_dwordx4 %[w3], s[60:61] offset:1024 sc1\n\t"
      "s_add_i32 s67, s67, 1\n\ts_lshr_b64 s[64:65], s[64:65], 4\n\ts_and_b32 s60, s64, 15\n\ts_mulk_i32 s60, 0x3800\n\t"
      "s_add_u32 s60, s60, %[blo]\n\ts_addc_u32 s61, %[bhi], 0\n\ts_add_i32 s69, s69, 0xb800\n\t"
      "s_cmp_gt_u32 s69, %[lmax]\n\ts_cselect_b32 s69, %[l0], s69\n\ts_mov_b32 m0, s69\n\ts_add_i32 s66, s66, 1\n\t"
      "s_mov_b32 s70, 1\n\ts_cmp_gt_i32 s67, 14\n\ts_cbranch_scc1 .Lx%=\n\ts_cmp_gt_i32 s67, s68\n\t"
      "s_cbranch_vccz .La%=\n\ts_branch .Lb%=\n\t"
      ".Ls%=:\n\ts_cmp_lg_u32 s70, 0\n\ts_cbranch_scc1 .Lt%=\n\ts_waitcnt vmcnt(0)\n\t"
      ".Lt%=:\n\ts_waitcnt vmcnt(10)\n\tv_cmp_ne_u32_e64 s[62:63], %[fv], %[tag]\n\ts_bitset1_b32 s63, 20\n\t"
      "s_ff1_i32_b64 s68, s[62:63]\n\ts_lshr_b32 s68, s68, 2\n\ts_add_i32 s68, s68, 2\n\ts_add_i32 s62, s66, 2\n\t"
      "s_cmp_gt_i32 s67, s62\n\ts_cbranch_scc1 .Lw%=\n\t"
      ".Lu%=:\n\ts_cmp_le_i32 s67, s68\n\ts_cbranch_scc1 .Lv%=\n\ts_add_i32 s71, s71, 1\n\ts_mov_b32 s68, 16\n\t"
      "s_cmp_gt_u32 s71, %[smax]\n\ts_cbranch_scc1 .Lv%=\n\tglobal_load_dword %[fv], %[fvo], %[fl] sc1\n\t"
      "s_waitcnt vmcnt(0)\n\tv_cmp_ne_u32_e64 s[62:63], %[fv], %[tag]\n\ts_bitset1_b32 s63, 20\n\t"
      "s_ff1_i32_b64 s68, s[62:63]\n\ts_lshr_b32 s68, s68, 2\n\ts_add_i32 s68, s68, 2\n\ts_branch .Lu%=\n\t"
      ".Lv%=:\n\tglobal_load_lds_dwordx4 %[w0], s[60:61] sc1\n\t"
      "global_load_lds_dwordx4 %[w0], s[60:61] offset:1024 sc1\n\t"
      "global_load_lds_dwordx4 %[w0], s[60:61] offset:2048 sc1\n\t"
      "global_load_lds_dwordx4 %[w0], s[60:61] offset:3072 sc1\n\ts_add_u32 m0, m0, 0x1000\n\ts_nop 0\n\t"
      "global_load_lds_dwordx4 %[w1], s[60:61] sc1\n\tglobal_load_lds_dwordx4 %[w1], s[60:61] offset:1024 sc1\n\t"
      "global_load_lds_dwordx4 %[w1], s[60:61] offset:2048 sc1\n\t"
      "global_load_lds_dwordx4 %[w1], s[60:61] offset:3072 sc1\n\ts_add_u32 m0, m0, 0x1000\n\ts_nop 0\n\t"
      "global_load_lds_dwordx4 %[w2], s[60:61] sc1\n\tglobal_load_lds_dwordx4 %[w2], s[60:61] offset:1024 sc1\n\t"
      "global_load_lds_dwordx4 %[w2], s[60:61] offset:2048 sc1\n\t"
      "global_load_lds_dwordx4 %[w2], s[60:61] offset:3072 sc1\n\ts_add_u32 m0, m0, 0x1000\n\ts_nop 0\n\t"
      "global_load_lds_dwordx4 %[w3], s[60:61] sc1\n\tglobal_load_lds_dwordx4 %[w3], s[60:61] offset:1024 sc1\n\t"
      "s_add_i32 s67, s67, 1\n\ts_lshr_b64 s[64:65], s[64:65], 4\n\ts_and_b32 s60, s64, 15\n\ts_mulk_i32 s60, 0x3800\n\t"
      "s_add_u32 s60, s60, %[blo]\n\ts_addc_u32 s61, %[bhi], 0\n\ts_add_i32 s69, s69, 0xb800\n\t"
      "s_cmp_gt_u32 s69, %[lmax]\n\ts_cselect_b32 s69, %[l0], s69\n\ts_mov_b32 m0, s69\n\ts_cmp_gt_i32 s67, 14\n\t"
      "s_cbranch_scc1 .Ly%=\n\t"
      ".Lw%=:\n\ts_mov_b64 vcc, 0\n\ts_cmp_le_i32 s67, s68\n\ts_cbranch_scc1 .Lf%=\n\t"
      "global_load_dword %[fv], %[fvo], %[fl] sc1\n\ts_add_i32 s66, s66, 1\n\ts_mov_b32 s70, 0\n\ts_cmp_eq_u32 s70, 0\n\t"
      "s_waitcnt vmcnt(1)\n\ts_branch .Lb%=\n\t"
      ".Lx%=:\n\ts_waitcnt vmcnt(14)\n\t"
      "s_barrier\n\t"
      ".Ly%=:\n\ts_add_i32 s66, s66, 1\n\ts_cmp_lt_u32 s66, 14\n\ts_waitcnt vmcnt(0)\n\t"
      "s_barrier\n\ts_cbranch_scc1 .Ly%=\n\t"
      : [fv] "+v"(fv)
      : [tag] "v"(tag), [fvo] "v"(fvo), [w0] "v"(wv), [w1] "v"(wv2), [w2] "v"(wv3), [w3] "v"(wv4), [fl] "s"(flags), [ord] "s"(ord),
        [blo] "s"((unsigned)(size_t)wbase), [bhi] "s"((unsigned)((size_t)wbase >> 32)), [l0] "s"(ldsw), [lmax] "s"(ldsw + 2 * SB), [smax] "s"(SPIN_MAX)
      : "memory", "scc", "vcc", "s60", "s61", "s62", "s63", "s64", "s65", "s66", "s67", "s68", "s69", "s70", "s71");
   } else {
#define SET_RDY(f_) do { const unsigned long long b_ = __ballot((f_) != tag);                                                     \
      rdy = 2 + ((int)__builtin_ctzll(b_ | (1ull << 52)) >> 2); } while (0)
#define POLL_BLOCK() do { unsigned f_; asm volatile("global_load_dword %0, %1, %2 sc1\n\ts_waitcnt vmcnt(0)" : "=&v"(f_) : "v"(fvo), "s"(flags) : "memory"); \
      SET_RDY(f_); } while (0)
    const char* b7 = wbase + KS(NT - 1) * WB; const unsigned l7 = (unsigned)(size_t)(shm) + STG_OFF;
    BAR();
    BAR();
#pragma unroll 1
    for (int j = 0; j < 4; j++) PUBLISH(STG_OFF + 2 * j * KCB, k2, j);
    BAR();
    WAIT_V(0);
    if (lane < 4) __hip_atomic_store(flags + 256 + tm * 32 + 4 + lane, tag, RLX_AGENT);
    BAR();
    BAR();
    BAR();
    BAR();
    asm volatile("global_load_dword %0, %1, %2 sc1" : "+v"(fv) : "v"(fvo), "s"(flags) : "memory");
    BAR();
    WAIT_V(0);
    asm volatile("" : "+v"(fv));
    int rdy; SET_RDY(fv);
    const bool ok = rdy == NT - 1;
    if (ok && tm == 0 && lane == 0) __hip_atomic_store(flags, tag, RLX_AGENT);
    if (ok) { __builtin_amdgcn_s_sleep(6); W1(wv, 0, l7 + 0, b7); W1(wv, 1024, l7 + 0, b7); W1(wv, 2048, l7 + 0, b7); W1(wv, 3072, l7 + 0, b7); }
    BAR();
    if (ok) { __builtin_amdgcn_s_sleep(6); W1(wv2, 0, l7 + 4096, b7); W1(wv2, 1024, l7 + 4096, b7); W1(wv2, 2048, l7 + 4096, b7); W1(wv2, 3072, l7 + 4096, b7); }
    BAR();
    if (ok) { __builtin_amdgcn_s_sleep(6); W1(wv3, 0, l7 + 8192, b7); W1(wv3, 1024, l7 + 8192, b7); W1(wv3, 2048, l7 + 8192, b7); W1(wv3, 3072, l7 + 8192, b7); }
    BAR();
    if (ok) { __builtin_amdgcn_s_sleep(6); W1(wv4, 0, l7 + 12288, b7); W1(wv4, 1024, l7 + 12288, b7); }
    BAR();
    if (__builtin_expect(!ok, 0)) {
      unsigned spins = 0;
#pragma unroll 1
      while (rdy < NT - 1) { if (++spins > SPIN_MAX) break; POLL_BLOCK(); }
      W4(wv, l7, b7); W4(wv2, l7 + 4096, b7); W4(wv3, l7 + 8192, b7); W2(wv4, l7 + 12288, b7);
      if (tm == 0 && lane == 0) __hip_atomic_store(flags, tag, RLX_AGENT);
    }
    BAR();
    WAIT_V(0);
    BAR();
   }
  }
#define UNIT(u_, a0_, o0_, a1_, o1_) u32x4 p0_, p1_; do { unsigned f_;                                                                 \
      do { asm volatile("ds_read_b32 %0, %3 offset:%8\n\tds_read_b128 %1, %4 offset:%6\n\tds_read_b128 %2, %5 offset:%7\n\ts_waitcnt lgkmcnt(0)" \
                        : "=&v"(f_), "=&v"(p0_), "=&v"(p1_) : "v"(fp), "v"(a0_), "v"(a1_), "n"(o0_), "n"(o1_), "n"(4 * (u_)) : "memory");        \
      } while (__builtin_amdgcn_readfirstlane(f_) == 0); } while (0)
#define UNIT1(u_, a0_, o0_) u32x4 p0_; do { unsigned f_;                                                                              \
      do { asm volatile("ds_read_b32 %0, %2 offset:%5\n\tds_read_b128 %1, %3 offset:%4\n\ts_waitcnt lgkmcnt(0)"                             \
                        : "=&v"(f_), "=&v"(p0_) : "v"(fp), "v"(a0_), "n"(o0_), "n"(4 * (u_)) : "memory");                                        \
      } while (__builtin_amdgcn_readfirstlane(f_) == 0); } while (0)
    { unsigned f_; do { asm volatile("ds_read_b32 %0, %1\n\ts_waitcnt lgkmcnt(0)" : "=v"(f_) : "v"(fp) : "memory"); } while (__builtin_amdgcn_readfirstlane(f_) == 0); }
    { UNIT(0, rw, 0, rw, 1024); O_ST(p0_, va[0], 0, AUX_NT); O_ST(p1_, va[1], 0, AUX_NT); }
    { UNIT(1, rw, 2048, rw, 3072); O_ST(p0_, va[2], 0, AUX_NT); O_ST(p1_, va[3], 0, AUX_NT); }
    { UNIT(2, rw, 4096, rw, 5120); O_ST(p0_, va[4], 0, AUX_NT); O_ST(p1_, va[5], 0, AUX_NT); }
    { UNIT(3, rw, 6144, rw, 7168); O_ST(p0_, va[6], 0, AUX_NT); O_ST(p1_, va[7], 0, AUX_NT); }
    { UNIT1(4, rs, 0); O_ST(p0_, va[8], 0, AUX_NT); }
    { UNIT1(5, rs, 2048); O_ST(p0_, va[9], 0, AUX_NT); }
    { UNIT1(6, rs, 4096); O_ST(p0_, va[10], 0, AUX_NT); }
    { UNIT1(7, rs, 6144); O_ST(p0_, va[11], 0, AUX_NT); }
  }
  asm volatile("" ::: "memory");
}

extern "C" void kernel_launch(void* const* d_in, const int* in_sizes, int n_in, void* d_out, int out_size, void* d_ws,
                              size_t ws_size, hipStream_t stream) {
  nf4_gemm<<<256, 512, 0, stream>>>((const uint16_t*)d_in[0], (const int*)d_in[1], (const uint16_t*)d_in[2],
                                    (const uint16_t*)d_in[3], (char*)d_ws, (uint16_t*)d_out);
}
```

```cpp
#include <hip/hip_runtime.h>
#include <hip/hip_bf16.h>
#include <stdint.h>

typedef __attribute__((ext_vector_type(8))) short bf16x8;
typedef __attribute__((ext_vector_type(4))) float f32x4;
typedef __attribute__((ext_vector_type(4))) int i32x4;
typedef __attribute__((ext_vector_type(4))) unsigned u32x4;
typedef __attribute__((ext_vector_type(2))) unsigned u32x2;
typedef __attribute__((ext_vector_type(2))) float f32x2;
typedef __attribute__((ext_vector_type(2))) __bf16 bf16x2_t;
typedef __attribute__((address_space(1))) unsigned gu32;

constexpr int M_ = 2048, K_ = 1024, N_ = 3584;
constexpr int BM = 256, BN = 112, BK = 64, NT = K_ / BK;
constexpr int XB = BM * BK * 2;
constexpr int WB = BN * BK * 2;
constexpr int KCB = BN * 16;
constexpr int SB = XB + WB;
constexpr int NS = 3;
constexpr int HDR = 4096;
constexpr int LUT_OFF = -HDR;
constexpr int FLG = 2560;
constexpr int CNT_OFF = -HDR + 2304;
constexpr int STG_OFF = NS * SB;
constexpr int SHM_BYTES = HDR + NS * SB + WB;
constexpr size_t WIMG_BYTES = (size_t)32 * NT * WB;
constexpr size_t SYNC_OFF = (size_t)8 << 20;
constexpr unsigned SPIN_MAX = 1u << 17;
constexpr unsigned CODE_TOUCH_MAX = 15604;
constexpr int AUX_NT = 2, AUX_SC1 = 16;

#define WAIT_V(n) asm volatile("s_waitcnt vmcnt(%0)" ::"n"(n) : "memory")
#define WAIT_L(n) asm volatile("s_waitcnt lgkmcnt(%0)" ::"n"(n) : "memory")
#define BAR() do { asm volatile("" ::: "memory"); __builtin_amdgcn_s_barrier(); asm volatile("" ::: "memory"); } while (0)
#define SCHED() __builtin_amdgcn_sched_barrier(0)
#define SGB(m_, n_) __builtin_amdgcn_sched_group_barrier(m_, n_, 0)
#define LGKM0() do { SCHED(); __builtin_amdgcn_s_waitcnt(0xC07F); SCHED(); } while (0)
#define TAG_OF(e) ((e) * 0x9E3779B1u + 0x7F4A7C15u)
#define RLX_AGENT __ATOMIC_RELAXED, __HIP_MEMORY_SCOPE_AGENT

__device__ __forceinline__ unsigned pack_bf16(float a, float b) {
  f32x2 v = {a, b};
  bf16x2_t r = __builtin_convertvector(v, bf16x2_t);
  return __builtin_bit_cast(unsigned, r);
}
__device__ __forceinline__ unsigned long long order_of(int tm, int tn) {
  const int t = tn & 15;
  const unsigned long long a = ((0x765432765432ull >> (8 * (t % 3))) & 0xffffff) ^ ((t & 1) * 0x111111);
  const unsigned long long b = (0xFEDCBA9FEDCBA9ull >> (4 * (t % 7))) & 0xfffffff;
  return (0x810 | a << 12 | b << 36) ^ (tm * 0x1111111111111111ull);
}

struct QRegs { i32x4 r[8]; };
__device__ __forceinline__ float smul(float a, float b) { float r; asm("v_mul_f32 %0, %1, %2" : "=v"(r) : "v"(a), "v"(b)); return r; }

__global__ __launch_bounds__(512, 2) void nf4_gemm(const uint16_t* __restrict__ x, const int* __restrict__ qw,
                                                   const uint16_t* __restrict__ cb, const uint16_t* __restrict__ am,
                                                   char* __restrict__ ws, uint16_t* __restrict__ out) {
  __shared__ __attribute__((aligned(1024))) char shm_raw[SHM_BYTES];
  char* const shm = shm_raw + HDR;
  unsigned long long pc0; asm volatile("s_getpc_b64 %0" : "=s"(pc0));
  unsigned ct;
  asm volatile("global_load_dword %0, %1, %2" : "=&v"(ct) : "v"(min((unsigned)((threadIdx.x & 7) * 2048 + ((blockIdx.x >> 3) & 15) * 128), CODE_TOUCH_MAX)), "s"(pc0) : "memory");
  const int bid = blockIdx.x;
  const int xcd = bid & 7, bi = bid >> 3;
  const int tn = (xcd >> 2) * 16 + (bi & 15), tm = (xcd & 3) * 2 + ((bi >> 4) & 1);
  const int k1 = tm ^ 1, k2 = tm + 8;
  const int tid = threadIdx.x, lane = tid & 63;
  const int wid = __builtin_amdgcn_readfirstlane(tid >> 6);
  const bool qact = (lane & 31) < 28;
  const int nc = qact ? (lane & 31) : 27;
  const int kc0 = 2 * (wid & 3);
  const int tj = tn & 3, cb0 = (tn >> 2) * 448;
  const int cw = tj * 64, cs = 256 + tj * 32, cq = 384 + (tj ^ 2) * 16;
  const int col = nc * 4 + (nc < 16 ? cw : nc < 24 ? cs - 64 : cq - 96);
  const unsigned qvoff = (unsigned)(((kc0 + (lane >> 5)) * 8 * N_ + col) * 4);
  const unsigned avoff = (unsigned)(col * 2);
  const unsigned qs0 = (unsigned)(cb0 * 4);
  const int ci = (int)(0x7688854328888180ull >> (4 * (tn & 15)));
  const unsigned xtv = (unsigned)((((lane & 7) * (K_ * 2) + (((0x810 >> (4 * min(lane >> 3, 2))) & 15) ^ tm) * 128) & (ci & 8 ? 0 : -1)) + (tm * BM + (ci & 7) * 32 + (wid & 3) * 8) * (K_ * 2));
  asm volatile("" :: "v"(qvoff), "v"(avoff), "v"(xtv));
  asm volatile("" :: "s"(qs0 + (unsigned)tm * (BK * N_ * 4)));
  SCHED();
  asm volatile("" :: "s"(x), "s"(qw), "s"(cb), "s"(am), "s"(ws), "s"(out));
  gu32* flags = (gu32*)(ws + SYNC_OFF + (size_t)tn * 2048);
  typedef __attribute__((address_space(3))) unsigned lu32;
  lu32* cnt = (lu32*)(shm + CNT_OFF);
#define SIGNAL(v_) asm volatile("s_mov_b64 exec, 1\n\tds_add_u32 %0, %1\n\ts_mov_b64 exec, -1" :: "v"(cnt), "v"(v_) : "memory")
#define AWAIT(m_, n_) do { while ((__hip_atomic_load(cnt, __ATOMIC_RELAXED, __HIP_MEMORY_SCOPE_WORKGROUP) & (m_)) < (n_)) {}     \
      asm volatile("" ::: "memory"); SCHED(); } while (0)
  char* lut = shm + LUT_OFF;
  const __amdgpu_buffer_rsrc_t rsW = __builtin_amdgcn_make_buffer_rsrc((void*)ws, (short)0, (int)WIMG_BYTES, 0x00020000);
  const __amdgpu_buffer_rsrc_t rsQ = __builtin_amdgcn_make_buffer_rsrc((void*)qw, (short)0, K_ * N_ * 4, 0x00020000);
  const uint16_t* am0 = am + cb0;
  const unsigned zero = 0;
#define Q_ROW(Q, j, so_) asm volatile("buffer_load_dwordx4 %0, %1, %2, %3 offen" : "=&v"(Q.r[j]) : "v"(qvoff), "s"(rsQ), "s"((so_) + (j) * (N_ * 4)) : "memory")
#define Q_ROWS(Q, so_) do { _Pragma("unroll") for (int j_ = 0; j_ < 8; j_++) Q_ROW(Q, j_, so_); } while (0)
#define Q_SC(S, ks) asm volatile("global_load_dwordx2 %0, %1, %2" : "=&v"(S) : "v"(avoff), "s"(am0 + (ks) * N_) : "memory")
#define Q_SO(ks) (qs0 + (unsigned)(ks) * (BK * N_ * 4))
#define TAG_LOAD(T) asm volatile("global_load_dword %0, %1, %2 sc1" : "=&v"(T) : "v"(zero), "s"(flags) : "memory")
#define SCALES(S) do { asm volatile("" : "+v"(S));                                                                                \
      sc[0] = __builtin_bit_cast(float, S.x << 16); sc[1] = __builtin_bit_cast(float, S.x & 0xffff0000u);                         \
      sc[2] = __builtin_bit_cast(float, S.y << 16); sc[3] = __builtin_bit_cast(float, S.y & 0xffff0000u); } while (0)
#define R_LOOK(Q, a_, n_) do { WAIT_V(n_); asm volatile("" : "+v"(Q.r[2 * (a_)]), "+v"(Q.r[2 * (a_) + 1]));                        \
      _Pragma("unroll") for (int i_ = 0; i_ < 4; i_ += 2) { u32x2 d_;                                                             \
        asm("v_lshl_add_u64 %0, %1, 4, %2\n\tv_lshl_add_u64 %0, %0, 3, 0" : "=&v"(d_)                                            \
            : "v"((u32x2){(unsigned)Q.r[2 * (a_) + 1][i_], (unsigned)Q.r[2 * (a_) + 1][i_ + 1]}),                                 \
              "v"((u32x2){(unsigned)Q.r[2 * (a_)][i_], (unsigned)Q.r[2 * (a_)][i_ + 1]}));                                        \
        lv[i_][a_] = *(const f32x2*)(lut + d_.x); lv[i_ + 1][a_] = *(const f32x2*)(lut + d_.y); }                                 \
      SCHED(); } while (0)
#define R_PACK(a_, sm) do { _Pragma("unroll") for (int i_ = 0; i_ < 4; i_++)                                                      \
      po[i_][a_] = (sm) ? pack_bf16(smul(lv[i_][a_].x, sc[i_]), smul(lv[i_][a_].y, sc[i_])) : pack_bf16(lv[i_][a_].x * sc[i_], lv[i_][a_].y * sc[i_]); \
      SCHED(); } while (0)
#define R_WRITE(dst) do { _Pragma("unroll") for (int i_ = 0; i_ < 4; i_++) *(u32x4*)(shm + (dst) + (i_ < 2 ? io0 : io1) + i_ * 896) = po[i_]; SCHED(); } while (0)
#define ROUND(Q, S, dst, e, sm) do { R_LOOK(Q, 0, (e) + 6); SCALES(S); R_LOOK(Q, 1, (e) + 4); R_PACK(0, sm);                      \
      R_LOOK(Q, 2, (e) + 2); R_PACK(1, sm); R_LOOK(Q, 3, e); R_PACK(2, sm); R_PACK(3, sm); R_WRITE(dst); } while (0)
#define PUBLISH(src, ks, kcp) do { WAIT_L(0); if (lane < 56) {                                                                    \
        const unsigned g_ = (unsigned)((tn * NT + (ks)) * WB + (kcp) * (2 * KCB) + lane * 16); u32x4 pv_[4];                      \
        _Pragma("unroll") for (int i_ = 0; i_ < 4; i_++) pv_[i_] = *(const u32x4*)(shm + (src) + lane * 16 + i_ * 896);           \
        _Pragma("unroll") for (int i_ = 0; i_ < 4; i_++) __builtin_amdgcn_raw_buffer_store_b128(pv_[i_], rsW, g_ + i_ * 896, 0, AUX_SC1); } \
      SCHED(); } while (0)
  f32x2 lv[4][4]; float sc[4]; u32x4 po[4];
  const __amdgpu_buffer_rsrc_t rsO = __builtin_amdgcn_make_buffer_rsrc((void*)out, (short)0, M_ * N_ * 2, 0x00020000);
#define O_ADDR(vo, ro, k) unsigned vo; int ro; do { const int r_ = lane >> (3 - (k)), c_ = lane & (7 >> (k));                     \
      vo = (unsigned)(((tm * BM + (wid & 3) * 64 + r_) * N_ + cb0 + ((k) == 0 ? cw : (k) == 1 ? cs : cq)) * 2 + c_ * 16);     \
      ro = HDR + ((k) ? 0 : SB) + (wid & 3) * 8192 + r_ * 128 + (((c_ + ((k) & 2) * 2) ^ (r_ & 7)) << 4);                         \
      asm volatile("" : "+v"(vo), "+v"(ro)); } while (0)
#define O_RD(ro, r_) (*(const u32x4*)(shm_raw + (ro) + (r_) * 128))
#define O_ST(v_, vo, r_, aux) __builtin_amdgcn_raw_buffer_store_b128(v_, rsO, (vo) + (r_) * (N_ * 2), 0, aux)
#define IO_OF() const int io0 = nc * 16 + (lane >> 5) * 448, io1 = nc * 16 + 448 - (lane >> 5) * 448

  if (__builtin_expect(wid < 4, 1)) {
    QRegs q; u32x2 qsv; unsigned c0, c1, tagv, xt0;
    asm volatile("global_load_ushort %0, %2, %4\n\tglobal_load_ushort %1, %3, %4" : "=&v"(c0), "=&v"(c1)
                 : "v"((unsigned)((lane & 15) * 2)), "v"((unsigned)((wid * 4 + (lane >> 4)) * 2)), "s"(cb) : "memory");
    TAG_LOAD(tagv);
    Q_SC(qsv, tm);
    asm volatile("global_load_dword %0, %1, %2" : "=&v"(xt0) : "v"(xtv), "s"(x) : "memory");
    Q_ROWS(q, Q_SO(tm));
    SCHED();
    IO_OF();
    const unsigned gb = (unsigned)((tn * NT + tm) * WB + kc0 * KCB);
    unsigned go0 = qact ? gb + io0 : 0x7fffffffu, go1 = qact ? gb + io1 : 0x7fffffffu;
    asm volatile("" : "+v"(go0), "+v"(go1));
    f32x4 acc[4][7];
#pragma unroll
    for (int m = 0; m < 4; m++)
#pragma unroll
      for (int n = 0; n < 7; n++) { acc[m][n] = (f32x4){0.f, 0.f, 0.f, 0.f}; asm volatile("" : "+v"(acc[m][n])); }
    WAIT_V(11);
    asm volatile("" : "+v"(c0), "+v"(c1)); asm volatile("" :: "v"(ct));
    *cnt = 0;
    *(u32x2*)(lut + tid * 8) = (u32x2){c0 << 16, c1 << 16};
    int fa = FLG + wid * 320 + lane * 4;
    asm volatile("" : "+v"(fa));
    *(unsigned*)(shm_raw + fa + 64) = 0u;
    WAIT_L(0);
    BAR();
    ROUND(q, qsv, XB + kc0 * KCB, 0, 0);
    WAIT_L(0);
    SIGNAL(1u);
    SCHED();
    __builtin_amdgcn_raw_buffer_store_b128(po[0], rsW, go0, 0, AUX_SC1);
    __builtin_amdgcn_raw_buffer_store_b128(po[1], rsW, go0 + 896, 0, AUX_SC1);
    __builtin_amdgcn_raw_buffer_store_b128(po[2], rsW, go1 + 2 * 896, 0, AUX_SC1);
    __builtin_amdgcn_raw_buffer_store_b128(po[3], rsW, go1 + 3 * 896, 0, AUX_SC1);
    SCHED();
    const int fr = lane & 15, fq = lane >> 4;
    const int xo0 = (wid * 64 + fr) * 128 + ((fq ^ (fr >> 1)) << 4), xo1 = xo0 ^ 64;
    const int wo = XB + (fq >> 1) * (2 * KCB) + (fr & 3) * 896 + ((fq ^ (fr >> 1)) & 1) * 448 + (fr >> 2) * 16;
    bf16x8 xa[4], wa[7], xb[4], wb[7];
#define RD(xf, wf, aw_, ax_, hf) do {                                                                 \
      _Pragma("unroll") for (int n_ = 0; n_ < 7; n_++) wf[n_] = *(const bf16x8*)(shm + (aw_) + (hf) * (4 * KCB) + n_ * 64); \
      _Pragma("unroll") for (int m_ = 0; m_ < 4; m_++) xf[m_] = *(const bf16x8*)(shm + (ax_) + m_ * 2048); } while (0)
#define MMR(xf, wf, i0, i1) do { _Pragma("unroll") for (int i_ = (i0); i_ < (i1); i_++) {             \
      const int m_ = i_ / 7, n_ = m_ & 1 ? 6 - i_ % 7 : i_ % 7;                                       \
      acc[m_][n_] = __builtin_amdgcn_mfma_f32_16x16x32_bf16(wf[n_], xf[m_], acc[m_][n_], 0, 0, 0); } } while (0)
    O_ADDR(vq, rq, 2);
    int e0 = HDR + wid * 8192 + fr * 128 + (((fq >> 1) ^ (fr & 7)) << 4) + (fq & 1) * 8, e1 = e0 ^ 32, e2 = e0 ^ 64, e3 = e0 ^ 96;
    asm volatile("" : "+v"(e0), "+v"(e1), "+v"(e2), "+v"(e3));
    AWAIT(15u, 6u);
#define INTER11() do { _Pragma("unroll") for (int i_ = 0; i_ < 11; i_++) { SGB(0x8, 2); SGB(0x100, 1); } } while (0)
#define STAGE(st) do { int aw_ = wo + (st) * SB; asm volatile("" : "+v"(aw_));                                                    \
      RD(xa, wa, aw_, xo0 + (st) * SB, 0);                                                                                        \
      SCHED();                                                                                                                    \
      RD(xb, wb, aw_, xo1 + (st) * SB, 1);                                                                                        \
      MMR(xa, wa, 0, 28);                                                                                                         \
      INTER11(); SGB(0x8, 6);                                                                                                     \
      SCHED();                                                                                                                    \
      if ((st) == 0) {                                                          \
        WAIT_V(0); asm volatile("" : "+v"(tagv) : "v"(xt0));                                                                      \
        if (lane == 0) __hip_atomic_store(flags + 256 + tm * 32 + wid, TAG_OF(tagv), RLX_AGENT); }                                \
      MMR(xb, wb, 0, 14);                                                                                                         \
      SCHED();                                                                                                                    \
      MMR(xb, wb, 14, 28);                                                                                                        \
      SCHED(); } while (0)
    STAGE(0);
    BAR();
    STAGE(1);
    asm volatile(".rept 4\n\ts_nop 0\n\t.endr");
    BAR();
    int aw = wo + STG_OFF - XB, a0 = xo0 + 2 * SB, a1 = xo1 + 2 * SB, so = 2 * SB;
    RD(xa, wa, aw, a0, 0);
    asm volatile("" : "+v"(aw), "+v"(a1));
#define HALF_A() do { SCHED();                                                                                                    \
      RD(xb, wb, aw, a1, 1);                                                                                                      \
      MMR(xa, wa, 0, 27);                                                                                                         \
      so = so == 2 * SB ? 0 : so + SB;                                                                                            \
      aw = wo + so; a0 = xo0 + so; a1 = xo1 + so;                                                                                 \
      INTER11(); SGB(0x8, 1); SGB(0x4, 3); SGB(0x8, 1); SGB(0x2, 2); SGB(0x8, 1); SGB(0x2, 1); SGB(0x8, 2);                       \
      asm volatile("" : "+v"(aw), "+v"(a0), "+v"(a1));                                                                            \
      LGKM0();                                                                                                                    \
      MMR(xa, wa, 27, 28);                                                                                                        \
      SCHED();                                                                                                                    \
      BAR();                                                                                                             \
      SCHED(); } while (0)
#pragma unroll 1
    for (int t = 2; t < NT - 3; t++) {
      HALF_A();
      RD(xa, wa, aw, a0, 0);
      MMR(xb, wb, 0, 28);
      INTER11(); SGB(0x8, 4); SGB(0x4, 2); SGB(0x8, 2);
    }
    HALF_A();
    SCHED();
#define LD(o_) (*(const bf16x8*)(shm + (o_)))
#define MF(m_, n_, xv, wf) do { asm volatile("v_mfma_f32_16x16x32_bf16 %0, %1, %2, %0" : "+v"(acc[m_][n_]) : "v"(wf[n_]), "v"(xv)); SCHED(); } while (0)
#define RX(xf, b_, m_) xf[m_] = LD((b_) + (m_) * 2048)
#define RW(wf, b_, hf, n_) wf[n_] = LD((b_) + (hf) * (4 * KCB) + (n_) * 64)
#define CV(m_, j_) pk[m_][j_] = pack_bf16(acc[m_][(j_) / 2][((j_) & 1) * 2], acc[m_][(j_) / 2][((j_) & 1) * 2 + 1])
#define WL(n_) do { SCHED(); __builtin_amdgcn_s_waitcnt(0xC07F | ((n_) << 8)); SCHED(); } while (0)
#define W64(m_, n_) *(u32x2*)(shm_raw + (((n_) & 3) == 0 ? e0 : ((n_) & 3) == 1 ? e1 : ((n_) & 3) == 2 ? e2 : e3) + ((n_) < 4 ? SB : 0) + (m_) * 2048) = (u32x2){pk[m_][2 * (n_)], pk[m_][2 * (n_) + 1]}
#define W32(m_, n_, h_) *(unsigned*)(shm_raw + (((n_) & 3) == 0 ? e0 : ((n_) & 3) == 1 ? e1 : ((n_) & 3) == 2 ? e2 : e3) + ((n_) < 4 ? SB : 0) + (m_) * 2048 + 4 * (h_)) = pk[m_][2 * (n_) + (h_)]
#define WA(m_, n_) W32(m_, n_, 0)
#define WB_(m_, n_) W32(m_, n_, 1)
#define FLAG(u_) do { asm volatile("" ::: "memory"); *(int*)(shm_raw + fa + 4 * (u_)) = fa; asm volatile("" ::: "memory"); } while (0)
    {
      bf16x8 xc[4], xd[4], xe[4], wc[7], wd[7], we[7];
      unsigned pk[4][14];
      u32x4 ov[2];
      MF(0, 0, xb[0], wb); RX(xa, a0, 0); SCHED();
      MF(0, 1, xb[0], wb); RW(wa, aw, 0, 0); SCHED();
      MF(0, 2, xb[0], wb); RW(wa, aw, 0, 1); SCHED();
      MF(0, 3, xb[0], wb); RW(wa, aw, 0, 2); SCHED();
      MF(1, 0, xb[1], wb); RW(wa, aw, 0, 3); SCHED();
      MF(1, 1, xb[1], wb); RX(xc, a1, 0); SCHED();
      MF(1, 2, xb[1], wb); RW(wc, aw, 1, 0); SCHED();
      MF(1, 3, xb[1], wb); RW(wc, aw, 1, 1); SCHED();
      MF(2, 0, xb[2], wb); RW(wc, aw, 1, 2); SCHED();
      MF(2, 1, xb[2], wb); RW(wc, aw, 1, 3); SCHED();
      MF(2, 2, xb[2], wb); RX(xa, a0, 1); SCHED();
      MF(2, 3, xb[2], wb); RX(xc, a1, 1); SCHED();
      MF(3, 0, xb[3], wb);
      MF(3, 1, xb[3], wb); RX(xa, a0, 2); SCHED();
      BAR();
      SCHED();
      MF(3, 2, xb[3], wb); RX(xd, xo0, 0); SCHED();
      MF(3, 3, xb[3], wb); RW(wd, aw + WB, 0, 0); WL(4); SCHED();
      MF(0, 0, xa[0], wa); RX(xe, xo1, 0); SCHED();
      MF(0, 1, xa[0], wa); RW(we, aw + WB, 1, 0); SCHED();
      MF(0, 2, xa[0], wa); RW(wd, aw + WB, 0, 1); SCHED();
      MF(0, 3, xa[0], wa); RW(we, aw + WB, 1, 1); SCHED();
      MF(0, 0, xc[0], wc); RW(wd, aw + WB, 0, 2); RW(we, aw + WB, 1, 2); SCHED();
      MF(0, 1, xc[0], wc); RW(wd, aw + WB, 0, 3); RW(we, aw + WB, 1, 3); SCHED();
      MF(0, 2, xc[0], wc); RX(xd, xo0, 1); RX(xe, xo1, 1); SCHED();
      MF(0, 3, xc[0], wc); RX(xc, a1, 2); WL(8); SCHED();
      MF(0, 0, xd[0], wd); RX(xd, xo0, 2); SCHED();
      MF(0, 0, xe[0], we);
      MF(0, 1, xd[0], wd); WL(4); SCHED();
      MF(0, 1, xe[0], we); WL(2); SCHED();
      MF(0, 2, xd[0], wd); CV(0, 0); CV(0, 1); SCHED();
      MF(0, 2, xe[0], we); W64(0, 0); SCHED();
      MF(0, 3, xd[0], wd); CV(0, 2); CV(0, 3); SCHED();
      MF(0, 3, xe[0], we); W64(0, 1); SCHED();
      MF(1, 0, xa[1], wa); CV(0, 4); CV(0, 5); SCHED();
      MF(1, 0, xc[1], wc); W64(0, 2); SCHED();
      MF(1, 0, xd[1], wd); CV(0, 6); CV(0, 7); SCHED();
      MF(1, 0, xe[1], we); W64(0, 3); SCHED();
      MF(1, 1, xa[1], wa); FLAG(1); SCHED();
      MF(1, 1, xc[1], wc); RX(xe, xo1, 2); RX(xa, a0, 3); SCHED();
      MF(1, 1, xd[1], wd); CV(1, 0); CV(1, 1); SCHED();
      MF(1, 1, xe[1], we); W64(1, 0); SCHED();
      MF(1, 2, xa[1], wa); RX(xc, a1, 3); RX(xd, xo0, 3); SCHED();
      MF(1, 2, xc[1], wc); RX(xe, xo1, 3); SCHED();
      MF(1, 2, xd[1], wd); CV(1, 2); CV(1, 3); SCHED();
      MF(1, 2, xe[1], we); W64(1, 1); SCHED();
      MF(1, 3, xa[1], wa); RW(wb, wo + SB, 1, 4); SCHED();
      MF(1, 3, xc[1], wc);
      MF(1, 3, xd[1], wd); CV(1, 4); CV(1, 5); SCHED();
      MF(1, 3, xe[1], we); W64(1, 2); SCHED();
      MF(2, 0, xa[2], wa); RW(wa, aw, 0, 4); WL(4); SCHED();
      MF(2, 0, xc[2], wc);
      MF(2, 0, xd[2], wd); CV(1, 6); CV(1, 7); SCHED();
      MF(2, 0, xe[2], we); W64(1, 3); SCHED();
      MF(2, 1, xa[2], wa); FLAG(2); SCHED();
      MF(2, 1, xc[2], wc); RW(wc, aw, 1, 4); SCHED();
      MF(2, 1, xd[2], wd); CV(2, 0); CV(2, 1); SCHED();
      MF(2, 1, xe[2], we); W64(2, 0); SCHED();
      MF(2, 2, xa[2], wa); RW(wd, aw + WB, 0, 4); SCHED();
      MF(2, 2, xc[2], wc);
      MF(2, 2, xd[2], wd); CV(2, 2); CV(2, 3); SCHED();
      MF(2, 2, xe[2], we); W64(2, 1); SCHED();
      MF(2, 3, xa[2], wa); RW(we, aw + WB, 1, 4); SCHED();
      MF(2, 3, xc[2], wc);
      MF(2, 3, xd[2], wd); CV(2, 4); CV(2, 5); SCHED();
      MF(2, 3, xe[2], we); W64(2, 2); SCHED();
      MF(3, 0, xa[3], wa); RW(wb, wo + SB, 1, 5); RW(wa, aw, 0, 5); SCHED();
      MF(3, 0, xc[3], wc); RW(wc, aw, 1, 5); SCHED();
      MF(3, 0, xd[3], wd); CV(2, 6); CV(2, 7); SCHED();
      MF(3, 0, xe[3], we); W64(2, 3); SCHED();
      MF(3, 1, xa[3], wa); FLAG(3); SCHED();
      MF(3, 1, xc[3], wc); RW(wd, aw + WB, 0, 5); RW(we, aw + WB, 1, 5); SCHED();
      MF(3, 1, xd[3], wd); CV(3, 0); CV(3, 1); SCHED();
      MF(3, 1, xe[3], we); W64(3, 0); SCHED();
      MF(3, 2, xa[3], wa);
      MF(3, 2, xc[3], wc);
      MF(3, 2, xd[3], wd); CV(3, 2); CV(3, 3); SCHED();
      MF(3, 2, xe[3], we); W64(3, 1); SCHED();
      MF(3, 3, xa[3], wa);
      MF(3, 3, xc[3], wc); WL(2); SCHED();
      MF(3, 3, xd[3], wd); CV(3, 4); CV(3, 5); SCHED();
      MF(3, 3, xe[3], we); W64(3, 2); SCHED();
      MF(0, 4, xb[0], wb); RW(wb, wo + SB, 1, 6); SCHED();
      MF(0, 4, xa[0], wa); RW(wa, aw, 0, 6); SCHED();
      MF(0, 4, xc[0], wc); CV(3, 6); CV(3, 7); SCHED();
      MF(0, 4, xd[0], wd); W64(3, 3); SCHED();
      MF(0, 4, xe[0], we); FLAG(4); SCHED();
      MF(0, 5, xb[0], wb); RW(wc, aw, 1, 6); RW(wd, aw + WB, 0, 6); SCHED();
      MF(0, 5, xa[0], wa); RW(we, aw + WB, 1, 6); SCHED();
      MF(0, 5, xc[0], wc); CV(0, 8); CV(0, 9); SCHED();
      MF(0, 5, xd[0], wd); WA(0, 4); SCHED();
      MF(0, 5, xe[0], we); WB_(0, 4); SCHED();
      MF(1, 4, xb[1], wb);
      MF(1, 4, xa[1], wa);
      MF(1, 4, xc[1], wc); CV(0, 10); CV(0, 11); SCHED();
      MF(1, 4, xd[1], wd); WA(0, 5); SCHED();
      MF(1, 4, xe[1], we); WB_(0, 5); SCHED();
      MF(1, 5, xb[1], wb); FLAG(5); SCHED();
      MF(1, 5, xa[1], wa);
      MF(1, 5, xc[1], wc); CV(1, 8); CV(1, 9); SCHED();
      MF(1, 5, xd[1], wd); WA(1, 4); SCHED();
      MF(1, 5, xe[1], we); WB_(1, 4); SCHED();
      MF(2, 4, xb[2], wb);
      MF(2, 4, xa[2], wa);
      MF(2, 4, xc[2], wc); CV(1, 10); CV(1, 11); SCHED();
      MF(2, 4, xd[2], wd); WA(1, 5); SCHED();
      MF(2, 4, xe[2], we); WB_(1, 5); SCHED();
      MF(2, 5, xb[2], wb); FLAG(6); SCHED();
      MF(2, 5, xa[2], wa);
      MF(2, 5, xc[2], wc); CV(2, 8); CV(2, 9); SCHED();
      MF(2, 5, xd[2], wd); WA(2, 4); SCHED();
      MF(2, 5, xe[2], we); WB_(2, 4); SCHED();
      MF(3, 4, xb[3], wb);
      MF(3, 4, xa[3], wa);
      MF(3, 4, xc[3], wc); CV(2, 10); CV(2, 11); SCHED();
      MF(3, 4, xd[3], wd); WA(2, 5); SCHED();
      MF(3, 4, xe[3], we); WB_(2, 5); SCHED();
      MF(3, 5, xb[3], wb); FLAG(7); SCHED();
      MF(3, 5, xa[3], wa); WL(15); SCHED();
      MF(3, 5, xc[3], wc); CV(3, 8); CV(3, 9); SCHED();
      MF(3, 5, xd[3], wd); WA(3, 4); SCHED();
      MF(3, 5, xe[3], we); WB_(3, 4); SCHED();
      MF(0, 6, xb[0], wb);
      MF(0, 6, xa[0], wa);
      MF(0, 6, xc[0], wc); CV(3, 10); CV(3, 11); SCHED();
      MF(0, 6, xd[0], wd); WA(3, 5); SCHED();
      MF(0, 6, xe[0], we); WB_(3, 5); SCHED();
      MF(1, 6, xb[1], wb); FLAG(8); SCHED();
      MF(1, 6, xa[1], wa);
      MF(1, 6, xc[1], wc); CV(0, 12); CV(0, 13); SCHED();
      MF(1, 6, xd[1], wd); WA(0, 6); SCHED();
      MF(1, 6, xe[1], we); WB_(0, 6); SCHED();
      MF(2, 6, xb[2], wb);
      MF(2, 6, xa[2], wa);
      MF(2, 6, xc[2], wc); CV(1, 12); CV(1, 13); SCHED();
      MF(2, 6, xd[2], wd); WA(1, 6); SCHED();
      MF(2, 6, xe[2], we); WB_(1, 6); SCHED();
      MF(3, 6, xb[3], wb); ov[0] = O_RD(rq, 0); SCHED();
      MF(3, 6, xa[3], wa);
      MF(3, 6, xc[3], wc); CV(2, 12); CV(2, 13); SCHED();
      MF(3, 6, xd[3], wd); WA(2, 6); SCHED();
      MF(3, 6, xe[3], we); WB_(2, 6); SCHED();
      O_ST(ov[0], vq, 0, 0); SCHED();
      asm volatile("s_nop 6");
      SCHED();
      CV(3, 12); CV(3, 13); W64(3, 6); ov[1] = O_RD(rq, 32); SCHED();
      O_ST(ov[1], vq, 32, 0);
    }
  } else {
    IO_OF();
    const unsigned long long ord = order_of(tm, tn);
#define KS(s_) ((int)(ord >> (4 * (s_))) & 15)
    const int h = wid & 1;
    unsigned xv[16];
#pragma unroll
    for (int i = 0; i < 16; i++) xv[i] = (unsigned)((8 * i + (lane >> 3)) * (K_ * 2) + (((lane & 7) ^ ((i & 1) * 4 + (lane >> 4))) << 4));
    const char* xbase = (const char*)(x + (size_t)(tm * BM + h * 128) * K_);
    const unsigned ldsx = (unsigned)(size_t)(shm) + h * 16384;
#define X8(o, l_, b_)                                                                                                             \
      asm volatile("s_mov_b32 m0, %8\n\ts_nop 0\n\tglobal_load_lds_dwordx4 %0, %9\n\t"                                            \
                   "s_add_u32 m0, m0, 0x400\n\ts_nop 0\n\tglobal_load_lds_dwordx4 %1, %9\n\t"                                     \
                   "s_add_u32 m0, m0, 0x400\n\ts_nop 0\n\tglobal_load_lds_dwordx4 %2, %9\n\t"                                     \
                   "s_add_u32 m0, m0, 0x400\n\ts_nop 0\n\tglobal_load_lds_dwordx4 %3, %9\n\t"                                     \
                   "s_add_u32 m0, m0, 0x400\n\ts_nop 0\n\tglobal_load_lds_dwordx4 %4, %9\n\t"                                     \
                   "s_add_u32 m0, m0, 0x400\n\ts_nop 0\n\tglobal_load_lds_dwordx4 %5, %9\n\t"                                     \
                   "s_add_u32 m0, m0, 0x400\n\ts_nop 0\n\tglobal_load_lds_dwordx4 %6, %9\n\t"                                     \
                   "s_add_u32 m0, m0, 0x400\n\ts_nop 0\n\tglobal_load_lds_dwordx4 %7, %9"                                         \
                   :: "v"(xv[o]), "v"(xv[o + 1]), "v"(xv[o + 2]), "v"(xv[o + 3]), "v"(xv[o + 4]), "v"(xv[o + 5]), "v"(xv[o + 6]), "v"(xv[o + 7]), \
                      "s"(l_), "s"(b_) : "memory", "scc")
#define ISSUE_X(s, slot) do { const char* b_ = xbase + KS(s) * 128; const unsigned l_ = ldsx + (slot) * SB;                       \
      X8(0, l_, b_); X8(8, l_ + 8192, b_); } while (0)
    const int fj = (int)(ord >> (4 * min(lane >> 2, 12) + 12));
    const unsigned fvo = (unsigned)(1024 + (fj & 7) * 128 + (fj & 8) * 2 + (lane & 3) * 4);
    O_ADDR(vw, rw, 0); O_ADDR(vs, rs, 1);
    unsigned fp = (unsigned)(size_t)shm_raw + FLG + (wid & 3) * 320 + 256;
    asm volatile("" : "+v"(fp));
    unsigned va[12];
#pragma unroll
    for (int i = 0; i < 12; i++) { va[i] = i < 8 ? vw + i * 8 * (N_ * 2) : vs + (i - 8) * 16 * (N_ * 2); asm volatile("" : "+v"(va[i])); }
  if (wid < 6) {
    QRegs qa, q; u32x2 sa, sb;
    Q_SC(sa, k1);
    Q_SC(sb, k2);
    BAR();
    ISSUE_X(0, 0);
    SIGNAL(0x1000u);
    Q_ROWS(qa, Q_SO(k1));
    WAIT_V(8);
    SIGNAL(1u);
    { const char* b_ = xbase + k1 * 128; const unsigned l_ = ldsx + SB; X8(0, l_, b_); X8(8, l_ + 8192, b_); }
    SIGNAL(16u);
    asm volatile("" :: "v"(ct));
    Q_ROWS(q, Q_SO(k2));
    ROUND(qa, sa, SB + XB + kc0 * KCB, 24, 1);
    WAIT_V(8);
    WAIT_L(0);
    BAR();
    X8(0, ldsx + 2 * SB, xbase + k2 * 128);
    ROUND(q, sb, STG_OFF + kc0 * KCB, 8, 1);
    X8(8, ldsx + 2 * SB + 8192, xbase + k2 * 128);
    WAIT_L(0);
    unsigned long long o = ord >> 12; unsigned l_ = ldsx;
#pragma unroll 1
    for (int t = 0; t < NT - 3; t++) {
      const char* b_ = xbase + ((unsigned)o & 15) * 128; unsigned l2 = l_ + 8192;
      asm volatile("" : "+s"(b_), "+s"(l_), "+s"(l2));
      if (t) { WAIT_V(16); BAR(); }
      X8(0, l_, b_); X8(8, l2, b_);
      o >>= 4; l_ = l_ == ldsx + 2 * SB ? ldsx : l_ + SB;
    }
    WAIT_V(16); BAR();
    WAIT_V(0); BAR();
  } else {
    QRegs q, qa; u32x2 sa, sb; unsigned tagv;
    Q_SC(sa, k1); Q_SC(sb, k2); TAG_LOAD(tagv);
    BAR();
    AWAIT(0xf000u, 0x2000u);
    Q_ROWS(qa, Q_SO(k1));
    ROUND(qa, sa, SB + XB + kc0 * KCB, 0, 1);
    AWAIT(0xf0u, 32u);
    Q_ROWS(q, Q_SO(k2));
    asm volatile("" :: "v"(ct));
    WAIT_L(0);
    asm volatile("" : "+v"(tagv));
    const unsigned tag = TAG_OF(tagv);
    unsigned fv = tagv;
    const unsigned fvo6 = wid == 6 ? fvo : 0u;
    BAR();
    asm volatile("global_load_dword %0, %1, %2 sc1" : "+v"(fv) : "v"(fvo6), "s"(flags) : "memory");
    ROUND(q, sb, STG_OFF + kc0 * KCB, 1, 1);
    WAIT_L(0);
    const char* wbase = ws + (size_t)tn * NT * WB;
    const unsigned wv = (unsigned)(lane * 16), wv2 = wv + 4096, wv3 = wv + 8192, wv4 = wv + 12288;
    const unsigned ldsw = (unsigned)(size_t)(shm) + XB;
#define W4(v_, l_, b_) asm volatile("s_mov_b32 m0, %1\n\ts_nop 0\n\t"                                                             \
                   "global_load_lds_dwordx4 %0, %2 sc1\n\tglobal_load_lds_dwordx4 %0, %2 offset:1024 sc1\n\t"                     \
                   "global_load_lds_dwordx4 %0, %2 offset:2048 sc1\n\tglobal_load_lds_dwordx4 %0, %2 offset:3072 sc1" :: "v"(v_), "s"(l_), "s"(b_) : "memory")
#define W2(v_, l_, b_) asm volatile("s_mov_b32 m0, %1\n\ts_nop 0\n\t"                                                             \
                   "global_load_lds_dwordx4 %0, %2 sc1\n\tglobal_load_lds_dwordx4 %0, %2 offset:1024 sc1" :: "v"(v_), "s"(l_), "s"(b_) : "memory")
#define W1(v_, o_, l_, b_) asm volatile("s_mov_b32 m0, %1\n\ts_nop 0\n\tglobal_load_lds_dwordx4 %0, %2 offset:%3 sc1" :: "v"(v_), "s"(l_), "s"(b_), "n"(o_) : "memory")
   if (__builtin_expect(wid == 6, 1)) {
    asm volatile(
      "s_lshr_b64 s[64:65], %[ord], 12\n\ts_mov_b32 s66, 0\n\ts_mov_b32 s67, 3\n\ts_mov_b32 s69, %[l0]\n\t"
      "s_mov_b32 s70, 0\n\ts_mov_b32 s71, 0\n\ts_mov_b32 m0, s69\n\ts_and_b32 s60, s64, 15\n\ts_mulk_i32 s60, 0x3800\n\t"
      "s_add_u32 s60, s60, %[blo]\n\ts_addc_u32 s61, %[bhi], 0\n\ts_waitcnt vmcnt(0)\n\t"
      "v_cmp_ne_u32_e64 s[62:63], %[fv], %[tag]\n\ts_bitset1_b32 s63, 20\n\ts_ff1_i32_b64 s68, s[62:63]\n\t"
      "s_lshr_b32 s68, s68, 2\n\ts_add_i32 s68, s68, 2\n\ts_mov_b64 vcc, 0\n\ts_cmp_gt_i32 s67, s68\n\t"
      "s_cbranch_scc1 .Lw%=\n\ts_branch .Lf%=\n\t"
      ".La%=:\n\ts_waitcnt vmcnt(14)\n\t"
      ".Lb%=:\n\ts_waitcnt vmcnt(15)\n\t"
      "s_barrier\n\ts_cbranch_scc1 .Ls%=\n\ts_sleep 5\n\t"
      ".Lf%=:\n\tglobal_load_lds_dwordx4 %[w0], s[60:61] sc1\n\t"
      "global_load_lds_dwordx4 %[w0], s[60:61] offset:1024 sc1\n\t"
      "global_load_lds_dwordx4 %[w0], s[60:61] offset:2048 sc1\n\t"
      "global_load_lds_dwordx4 %[w0], s[60:61] offset:3072 sc1\n\ts_cbranch_vccz .Lg%=\n\ts_waitcnt vmcnt(14)\n\t"
      "v_cmp_ne_u32_e64 s[62:63], %[fv], %[tag]\n\ts_bitset1_b32 s63, 20\n\ts_ff1_i32_b64 s68, s[62:63]\n\t"
      "s_lshr_b32 s68, s68, 2\n\ts_add_i32 s68, s68, 2\n\t"
      ".Lg%=:\n\ts_add_i32 s62, s66, 5\n\ts_min_i32 s62, s62, 15\n\ts_cmp_lt_i32 s68, s62\n\ts_cselect_b64 vcc, -1, 0\n\t"
      "s_cbranch_scc0 .Lh%=\n\tglobal_load_dword %[fv], %[fvo], %[fl] sc1\n\t"
      ".Lh%=:\n\ts_add_u32 m0, m0, 0x1000\n\ts_nop 0\n\tglobal_load_lds_dwordx4 %[w1], s[60:61] sc1\n\t"
      "global_load_lds_dwordx4 %[w1], s[60:61] offset:1024 sc1\n\t"
      "global_load_lds_dwordx4 %[w1], s[60:61] offset:2048 sc1\n\t"
      "global_load_lds_dwordx4 %[w1], s[60:61] offset:3072 sc1\n\ts_add_u32 m0, m0, 0x1000\n\ts_nop 0\n\t"
      "global_load_lds_dwordx4 %[w2], s[60:61] sc1\n\tglobal_load_lds_dwordx4 %[w2], s[60:61] offset:1024 sc1\n\t"
      "global_load_lds_dwordx4 %[w2], s[60:61] offset:2048 sc1\n\t"
      "global_load_lds_dwordx4 %[w2], s[60:61] offset:3072 sc1\n\ts_add_u32 m0, m0, 0x1000\n\ts_nop 0\n\t"
      "global_load_lds_dwordx4 %[w3], s[60:61] sc1\n\tglobal_load_lds_dwordx4 %[w3], s[60:61] offset:1024 sc1\n\t"
      "s_add_i32 s67, s67, 1\n\ts_lshr_b64 s[64:65], s[64:65], 4\n\ts_and_b32 s60, s64, 15\n\ts_mulk_i32 s60, 0x3800\n\t"
      "s_add_u32 s60, s60, %[blo]\n\ts_addc_u32 s61, %[bhi], 0\n\ts_add_i32 s69, s69, 0xb800\n\t"
      "s_cmp_gt_u32 s69, %[lmax]\n\ts_cselect_b32 s69, %[l0], s69\n\ts_mov_b32 m0, s69\n\ts_add_i32 s66, s66, 1\n\t"
      "s_mov_b32 s70, 1\n\ts_cmp_gt_i32 s67, 14\n\ts_cbranch_scc1 .Lx%=\n\ts_cmp_gt_i32 s67, s68\n\t"
      "s_cbranch_vccz .La%=\n\ts_branch .Lb%=\n\t"
      ".Ls%=:\n\ts_cmp_lg_u32 s70, 0\n\ts_cbranch_scc1 .Lt%=\n\ts_waitcnt vmcnt(0)\n\t"
      ".Lt%=:\n\ts_waitcnt vmcnt(10)\n\tv_cmp_ne_u32_e64 s[62:63], %[fv], %[tag]\n\ts_bitset1_b32 s63, 20\n\t"
      "s_ff1_i32_b64 s68, s[62:63]\n\ts_lshr_b32 s68, s68, 2\n\ts_add_i32 s68, s68, 2\n\ts_add_i32 s62, s66, 2\n\t"
      "s_cmp_gt_i32 s67, s62\n\ts_cbranch_scc1 .Lw%=\n\t"
      ".Lu%=:\n\ts_cmp_le_i32 s67, s68\n\ts_cbranch_scc1 .Lv%=\n\ts_add_i32 s71, s71, 1\n\ts_mov_b32 s68, 16\n\t"
      "s_cmp_gt_u32 s71, %[smax]\n\ts_cbranch_scc1 .Lv%=\n\tglobal_load_dword %[fv], %[fvo], %[fl] sc1\n\t"
      "s_waitcnt vmcnt(0)\n\tv_cmp_ne_u32_e64 s[62:63], %[fv], %[tag]\n\ts_bitset1_b32 s63, 20\n\t"
      "s_ff1_i32_b64 s68, s[62:63]\n\ts_lshr_b32 s68, s68, 2\n\ts_add_i32 s68, s68, 2\n\ts_branch .Lu%=\n\t"
      ".Lv%=:\n\tglobal_load_lds_dwordx4 %[w0], s[60:61] sc1\n\t"
      "global_load_lds_dwordx4 %[w0], s[60:61] offset:1024 sc1\n\t"
      "global_load_lds_dwordx4 %[w0], s[60:61] offset:2048 sc1\n\t"
      "global_load_lds_dwordx4 %[w0], s[60:61] offset:3072 sc1\n\ts_add_u32 m0, m0, 0x1000\n\ts_nop 0\n\t"
      "global_load_lds_dwordx4 %[w1], s[60:61] sc1\n\tglobal_load_lds_dwordx4 %[w1], s[60:61] offset:1024 sc1\n\t"
      "global_load_lds_dwordx4 %[w1], s[60:61] offset:2048 sc1\n\t"
      "global_load_lds_dwordx4 %[w1], s[60:61] offset:3072 sc1\n\ts_add_u32 m0, m0, 0x1000\n\ts_nop 0\n\t"
      "global_load_lds_dwordx4 %[w2], s[60:61] sc1\n\tglobal_load_lds_dwordx4 %[w2], s[60:61] offset:1024 sc1\n\t"
      "global_load_lds_dwordx4 %[w2], s[60:61] offset:2048 sc1\n\t"
      "global_load_lds_dwordx4 %[w2], s[60:61] offset:3072 sc1\n\ts_add_u32 m0, m0, 0x1000\n\ts_nop 0\n\t"
      "global_load_lds_dwordx4 %[w3], s[60:61] sc1\n\tglobal_load_lds_dwordx4 %[w3], s[60:61] offset:1024 sc1\n\t"
      "s_add_i32 s67, s67, 1\n\ts_lshr_b64 s[64:65], s[64:65], 4\n\ts_and_b32 s60, s64, 15\n\ts_mulk_i32 s60, 0x3800\n\t"
      "s_add_u32 s60, s60, %[blo]\n\ts_addc_u32 s61, %[bhi], 0\n\ts_add_i32 s69, s69, 0xb800\n\t"
      "s_cmp_gt_u32 s69, %[lmax]\n\ts_cselect_b32 s69, %[l0], s69\n\ts_mov_b32 m0, s69\n\ts_cmp_gt_i32 s67, 14\n\t"
      "s_cbranch_scc1 .Ly%=\n\t"
      ".Lw%=:\n\ts_mov_b64 vcc, 0\n\ts_cmp_le_i32 s67, s68\n\ts_cbranch_scc1 .Lf%=\n\t"
      "global_load_dword %[fv], %[fvo], %[fl] sc1\n\ts_add_i32 s66, s66, 1\n\ts_mov_b32 s70, 0\n\ts_cmp_eq_u32 s70, 0\n\t"
      "s_waitcnt vmcnt(1)\n\ts_branch .Lb%=\n\t"
      ".Lx%=:\n\ts_waitcnt vmcnt(14)\n\t"
      "s_barrier\n\t"
      ".Ly%=:\n\ts_add_i32 s66, s66, 1\n\ts_cmp_lt_u32 s66, 14\n\ts_waitcnt vmcnt(0)\n\t"
      "s_barrier\n\ts_cbranch_scc1 .Ly%=\n\t"
      : [fv] "+v"(fv)
      : [tag] "v"(tag), [fvo] "v"(fvo), [w0] "v"(wv), [w1] "v"(wv2), [w2] "v"(wv3), [w3] "v"(wv4), [fl] "s"(flags), [ord] "s"(ord),
        [blo] "s"((unsigned)(size_t)wbase), [bhi] "s"((unsigned)((size_t)wbase >> 32)), [l0] "s"(ldsw), [lmax] "s"(ldsw + 2 * SB), [smax] "s"(SPIN_MAX)
      : "memory", "scc", "vcc", "s60", "s61", "s62", "s63", "s64", "s65", "s66", "s67", "s68", "s69", "s70", "s71");
   } else {
#define SET_RDY(f_) do { const unsigned long long b_ = __ballot((f_) != tag);                                                     \
      rdy = 2 + ((int)__builtin_ctzll(b_ | (1ull << 52)) >> 2); } while (0)
#define POLL_BLOCK() do { unsigned f_; asm volatile("global_load_dword %0, %1, %2 sc1\n\ts_waitcnt vmcnt(0)" : "=&v"(f_) : "v"(fvo), "s"(flags) : "memory"); \
      SET_RDY(f_); } while (0)
    const char* b7 = wbase + KS(NT - 1) * WB; const unsigned l7 = (unsigned)(size_t)(shm) + STG_OFF;
    BAR();
#pragma unroll 1
    for (int j = 0; j < 2; j++) PUBLISH(STG_OFF + 2 * j * KCB, k2, j);
    BAR();
#pragma unroll 1
    for (int j = 2; j < 4; j++) PUBLISH(STG_OFF + 2 * j * KCB, k2, j);
    BAR();
    WAIT_V(0);
    if (lane < 4) __hip_atomic_store(flags + 256 + tm * 32 + 4 + lane, tag, RLX_AGENT);
    BAR();
    BAR();
    BAR();
    BAR();
    asm volatile("global_load_dword %0, %1, %2 sc1" : "+v"(fv) : "v"(fvo), "s"(flags) : "memory");
    BAR();
    WAIT_V(0);
    asm volatile("" : "+v"(fv));
    int rdy; SET_RDY(fv);
    const bool ok = rdy == NT - 1;
    if (ok && tm == 0 && lane == 0) __hip_atomic_store(flags, tag, RLX_AGENT);
    if (ok) { __builtin_amdgcn_s_sleep(6); W1(wv, 0, l7 + 0, b7); W1(wv, 1024, l7 + 0, b7); W1(wv, 2048, l7 + 0, b7); W1(wv, 3072, l7 + 0, b7); }
    BAR();
    if (ok) { __builtin_amdgcn_s_sleep(6); W1(wv2, 0, l7 + 4096, b7); W1(wv2, 1024, l7 + 4096, b7); W1(wv2, 2048, l7 + 4096, b7); W1(wv2, 3072, l7 + 4096, b7); }
    BAR();
    if (ok) { __builtin_amdgcn_s_sleep(6); W1(wv3, 0, l7 + 8192, b7); W1(wv3, 1024, l7 + 8192, b7); W1(wv3, 2048, l7 + 8192, b7); W1(wv3, 3072, l7 + 8192, b7); }
    BAR();
    if (ok) { __builtin_amdgcn_s_sleep(6); W1(wv4, 0, l7 + 12288, b7); W1(wv4, 1024, l7 + 12288, b7); }
    BAR();
    if (__builtin_expect(!ok, 0)) {
      unsigned spins = 0;
#pragma unroll 1
      while (rdy < NT - 1) { if (++spins > SPIN_MAX) break; POLL_BLOCK(); }
      W4(wv, l7, b7); W4(wv2, l7 + 4096, b7); W4(wv3, l7 + 8192, b7); W2(wv4, l7 + 12288, b7);
      if (tm == 0 && lane == 0) __hip_atomic_store(flags, tag, RLX_AGENT);
    }
    BAR();
    WAIT_V(0);
    BAR();
   }
  }
#define UNIT(u_, a0_, o0_, a1_, o1_) u32x4 p0_, p1_; do { unsigned f_;                                                                 \
      do { asm volatile("ds_read_b32 %0, %3 offset:%8\n\tds_read_b128 %1, %4 offset:%6\n\tds_read_b128 %2, %5 offset:%7\n\ts_waitcnt lgkmcnt(0)" \
                        : "=&v"(f_), "=&v"(p0_), "=&v"(p1_) : "v"(fp), "v"(a0_), "v"(a1_), "n"(o0_), "n"(o1_), "n"(4 * (u_)) : "memory");        \
      } while (__builtin_amdgcn_readfirstlane(f_) == 0); } while (0)
#define UNIT1(u_, a0_, o0_) u32x4 p0_; do { unsigned f_;                                                                              \
      do { asm volatile("ds_read_b32 %0, %2 offset:%5\n\tds_read_b128 %1, %3 offset:%4\n\ts_waitcnt lgkmcnt(0)"                             \
                        : "=&v"(f_), "=&v"(p0_) : "v"(fp), "v"(a0_), "n"(o0_), "n"(4 * (u_)) : "memory");                                        \
      } while (__builtin_amdgcn_readfirstlane(f_) == 0); } while (0)
    { unsigned f_; do { asm volatile("ds_read_b32 %0, %1\n\ts_waitcnt lgkmcnt(0)" : "=v"(f_) : "v"(fp) : "memory"); } while (__builtin_amdgcn_readfirstlane(f_) == 0); }
    { UNIT(0, rw, 0, rw, 1024); O_ST(p0_, va[0], 0, AUX_NT); O_ST(p1_, va[1], 0, AUX_NT); }
    { UNIT(1, rw, 2048, rw, 3072); O_ST(p0_, va[2], 0, AUX_NT); O_ST(p1_, va[3], 0, AUX_NT); }
    { UNIT(2, rw, 4096, rw, 5120); O_ST(p0_, va[4], 0, AUX_NT); O_ST(p1_, va[5], 0, AUX_NT); }
    { UNIT(3, rw, 6144, rw, 7168); O_ST(p0_, va[6], 0, AUX_NT); O_ST(p1_, va[7], 0, AUX_NT); }
    { UNIT1(4, rs, 0); O_ST(p0_, va[8], 0, AUX_NT); }
    { UNIT1(5, rs, 2048); O_ST(p0_, va[9], 0, AUX_NT); }
    { UNIT1(6, rs, 4096); O_ST(p0_, va[10], 0, AUX_NT); }
    { UNIT1(7, rs, 6144); O_ST(p0_, va[11], 0, AUX_NT); }
  }
  asm volatile("" ::: "memory");
}

extern "C" void kernel_launch(void* const* d_in, const int* in_sizes, int n_in, void* d_out, int out_size, void* d_ws,
                              size_t ws_size, hipStream_t stream) {
  nf4_gemm<<<256, 512, 0, stream>>>((const uint16_t*)d_in[0], (const int*)d_in[1], (const uint16_t*)d_in[2],
                                    (const uint16_t*)d_in[3], (char*)d_ws, (uint16_t*)d_out);
}
```

```cpp
#include <hip/hip_runtime.h>
#include <hip/hip_bf16.h>
#include <stdint.h>

typedef __attribute__((ext_vector_type(8))) short bf16x8;
typedef __attribute__((ext_vector_type(4))) float f32x4;
typedef __attribute__((ext_vector_type(4))) int i32x4;
typedef __attribute__((ext_vector_type(4))) unsigned u32x4;
typedef __attribute__((ext_vector_type(2))) unsigned u32x2;
typedef __attribute__((ext_vector_type(2))) float f32x2;
typedef __attribute__((ext_vector_type(2))) __bf16 bf16x2_t;
typedef __attribute__((address_space(1))) unsigned gu32;

constexpr int M_ = 2048, K_ = 1024, N_ = 3584;
constexpr int BM = 256, BN = 112, BK = 64, NT = K_ / BK;
constexpr int XB = BM * BK * 2;
constexpr int WB = BN * BK * 2;
constexpr int KCB = BN * 16;
constexpr int SB = XB + WB;
constexpr int NS = 3;
constexpr int HDR = 4096;
constexpr int LUT_OFF = -HDR;
constexpr int FLG = 2560;
constexpr int CNT_OFF = -HDR + 2304;
constexpr int STG_OFF = NS * SB;
constexpr int SHM_BYTES = HDR + NS * SB + WB;
constexpr size_t WIMG_BYTES = (size_t)32 * NT * WB;
constexpr size_t SYNC_OFF = (size_t)8 << 20;
constexpr unsigned SPIN_MAX = 1u << 17;
constexpr unsigned CODE_TOUCH_MAX = 15544;
constexpr int AUX_NT = 2, AUX_SC1 = 16;

#define WAIT_V(n) asm volatile("s_waitcnt vmcnt(%0)" ::"n"(n) : "memory")
#define WAIT_L(n) asm volatile("s_waitcnt lgkmcnt(%0)" ::"n"(n) : "memory")
#define BAR() do { asm volatile("" ::: "memory"); __builtin_amdgcn_s_barrier(); asm volatile("" ::: "memory"); } while (0)
#define SCHED() __builtin_amdgcn_sched_barrier(0)
#define TAG_OF(e) ((e) * 0x9E3779B1u + 0x7F4A7C15u)
#define RLX_AGENT __ATOMIC_RELAXED, __HIP_MEMORY_SCOPE_AGENT

__device__ __forceinline__ unsigned pack_bf16(float a, float b) {
  f32x2 v = {a, b};
  bf16x2_t r = __builtin_convertvector(v, bf16x2_t);
  return __builtin_bit_cast(unsigned, r);
}
__device__ __forceinline__ unsigned long long order_of(int tm, int tn) {
  const int t = tn & 15;
  const unsigned long long a = ((0x765432765432ull >> (8 * (t % 3))) & 0xffffff) ^ ((t & 1) * 0x111111);
  const unsigned long long b = (0xFEDCBA9FEDCBA9ull >> (4 * (t % 7))) & 0xfffffff;
  return (0x810 | a << 12 | b << 36) ^ (tm * 0x1111111111111111ull);
}

struct QRegs { i32x4 r[8]; };
__device__ __forceinline__ float smul(float a, float b) { float r; asm("v_mul_f32 %0, %1, %2" : "=v"(r) : "v"(a), "v"(b)); return r; }

__global__ __launch_bounds__(512, 2) void nf4_gemm(const uint16_t* __restrict__ x, const int* __restrict__ qw,
                                                   const uint16_t* __restrict__ cb, const uint16_t* __restrict__ am,
                                                   char* __restrict__ ws, uint16_t* __restrict__ out) {
  __shared__ __attribute__((aligned(1024))) char shm_raw[SHM_BYTES];
  char* const shm = shm_raw + HDR;
  unsigned long long pc0; asm volatile("s_getpc_b64 %0" : "=s"(pc0));
  unsigned ct;
  asm volatile("global_load_dword %0, %1, %2" : "=&v"(ct) : "v"(min((unsigned)((threadIdx.x & 7) * 2048 + ((blockIdx.x >> 3) & 15) * 128), CODE_TOUCH_MAX)), "s"(pc0) : "memory");
  const int bid = blockIdx.x;
  const int xcd = bid & 7, bi = bid >> 3;
  const int tn = (xcd >> 2) * 16 + (bi & 15), tm = (xcd & 3) * 2 + ((bi >> 4) & 1);
  const int k1 = tm ^ 1, k2 = tm + 8;
  const int tid = threadIdx.x, lane = tid & 63;
  const int wid = __builtin_amdgcn_readfirstlane(tid >> 6);
  const bool qact = (lane & 31) < 28;
  const int nc = qact ? (lane & 31) : 27;
  const int kc0 = 2 * (wid & 3);
  const int tj = tn & 3, cb0 = (tn >> 2) * 448;
  const int cw = tj * 64, cs = 256 + tj * 32, cq = 384 + (tj ^ 2) * 16;
  const int col = nc * 4 + (nc < 16 ? cw : nc < 24 ? cs - 64 : cq - 96);
  const unsigned qvoff = (unsigned)(((kc0 + (lane >> 5)) * 8 * N_ + col) * 4);
  const unsigned avoff = (unsigned)(col * 2);
  const unsigned qs0 = (unsigned)(cb0 * 4);
  const int ci = (int)(0x7688854328888180ull >> (4 * (tn & 15)));
  const unsigned xtv = (unsigned)((((lane & 7) * (K_ * 2) + (((0x810 >> (4 * min(lane >> 3, 2))) & 15) ^ tm) * 128) & (ci & 8 ? 0 : -1)) + (tm * BM + (ci & 7) * 32 + (wid & 3) * 8) * (K_ * 2));
  asm volatile("" :: "v"(qvoff), "v"(avoff), "v"(xtv));
  asm volatile("" :: "s"(qs0 + (unsigned)tm * (BK * N_ * 4)));
  SCHED();
  asm volatile("" :: "s"(x), "s"(qw), "s"(cb), "s"(am), "s"(ws), "s"(out));
  gu32* flags = (gu32*)(ws + SYNC_OFF + (size_t)tn * 2048);
  typedef __attribute__((address_space(3))) unsigned lu32;
  lu32* cnt = (lu32*)(shm + CNT_OFF);
#define SIGNAL(v_) asm volatile("s_mov_b64 exec, 1\n\tds_add_u32 %0, %1\n\ts_mov_b64 exec, -1" :: "v"(cnt), "v"(v_) : "memory")
#define AWAIT(m_, n_) do { while ((__hip_atomic_load(cnt, __ATOMIC_RELAXED, __HIP_MEMORY_SCOPE_WORKGROUP) & (m_)) < (n_)) {}     \
      asm volatile("" ::: "memory"); SCHED(); } while (0)
  char* lut = shm + LUT_OFF;
  const __amdgpu_buffer_rsrc_t rsW = __builtin_amdgcn_make_buffer_rsrc((void*)ws, (short)0, (int)WIMG_BYTES, 0x00020000);
  const __amdgpu_buffer_rsrc_t rsQ = __builtin_amdgcn_make_buffer_rsrc((void*)qw, (short)0, K_ * N_ * 4, 0x00020000);
  const uint16_t* am0 = am + cb0;
  const unsigned zero = 0;
#define Q_ROW(Q, j, so_) asm volatile("buffer_load_dwordx4 %0, %1, %2, %3 offen" : "=&v"(Q.r[j]) : "v"(qvoff), "s"(rsQ), "s"((so_) + (j) * (N_ * 4)) : "memory")
#define Q_ROWS(Q, so_) do { _Pragma("unroll") for (int j_ = 0; j_ < 8; j_++) Q_ROW(Q, j_, so_); } while (0)
#define Q_SC(S, ks) asm volatile("global_load_dwordx2 %0, %1, %2" : "=&v"(S) : "v"(avoff), "s"(am0 + (ks) * N_) : "memory")
#define Q_SO(ks) (qs0 + (unsigned)(ks) * (BK * N_ * 4))
#define TAG_LOAD(T) asm volatile("global_load_dword %0, %1, %2 sc1" : "=&v"(T) : "v"(zero), "s"(flags) : "memory")
#define SCALES(S) do { asm volatile("" : "+v"(S));                                                                                \
      sc[0] = __builtin_bit_cast(float, S.x << 16); sc[1] = __builtin_bit_cast(float, S.x & 0xffff0000u);                         \
      sc[2] = __builtin_bit_cast(float, S.y << 16); sc[3] = __builtin_bit_cast(float, S.y & 0xffff0000u); } while (0)
#define R_LOOK(Q, a_, n_) do { WAIT_V(n_); asm volatile("" : "+v"(Q.r[2 * (a_)]), "+v"(Q.r[2 * (a_) + 1]));                        \
      _Pragma("unroll") for (int i_ = 0; i_ < 4; i_ += 2) { u32x2 d_;                                                             \
        asm("v_lshl_add_u64 %0, %1, 4, %2\n\tv_lshl_add_u64 %0, %0, 3, 0" : "=&v"(d_)                                            \
            : "v"((u32x2){(unsigned)Q.r[2 * (a_) + 1][i_], (unsigned)Q.r[2 * (a_) + 1][i_ + 1]}),                                 \
              "v"((u32x2){(unsigned)Q.r[2 * (a_)][i_], (unsigned)Q.r[2 * (a_)][i_ + 1]}));                                        \
        lv[i_][a_] = *(const f32x2*)(lut + d_.x); lv[i_ + 1][a_] = *(const f32x2*)(lut + d_.y); }                                 \
      SCHED(); } while (0)
#define R_PACK(a_, sm) do { _Pragma("unroll") for (int i_ = 0; i_ < 4; i_++)                                                      \
      po[i_][a_] = (sm) ? pack_bf16(smul(lv[i_][a_].x, sc[i_]), smul(lv[i_][a_].y, sc[i_])) : pack_bf16(lv[i_][a_].x * sc[i_], lv[i_][a_].y * sc[i_]); \
      SCHED(); } while (0)
#define R_WRITE(dst) do { _Pragma("unroll") for (int i_ = 0; i_ < 4; i_++) *(u32x4*)(shm + (dst) + (i_ < 2 ? io0 : io1) + i_ * 896) = po[i_]; SCHED(); } while (0)
#define ROUND(Q, S, dst, e, sm) do { R_LOOK(Q, 0, (e) + 6); SCALES(S); R_LOOK(Q, 1, (e) + 4); R_PACK(0, sm);                      \
      R_LOOK(Q, 2, (e) + 2); R_PACK(1, sm); R_LOOK(Q, 3, e); R_PACK(2, sm); R_PACK(3, sm); R_WRITE(dst); } while (0)
#define PUBLISH(src, ks, kcp) do { WAIT_L(0); if (lane < 56) {                                                                    \
        const unsigned g_ = (unsigned)((tn * NT + (ks)) * WB + (kcp) * (2 * KCB) + lane * 16); u32x4 pv_[4];                      \
        _Pragma("unroll") for (int i_ = 0; i_ < 4; i_++) pv_[i_] = *(const u32x4*)(shm + (src) + lane * 16 + i_ * 896);           \
        _Pragma("unroll") for (int i_ = 0; i_ < 4; i_++) __builtin_amdgcn_raw_buffer_store_b128(pv_[i_], rsW, g_ + i_ * 896, 0, AUX_SC1); } \
      SCHED(); } while (0)
  f32x2 lv[4][4]; float sc[4]; u32x4 po[4];
  const __amdgpu_buffer_rsrc_t rsO = __builtin_amdgcn_make_buffer_rsrc((void*)out, (short)0, M_ * N_ * 2, 0x00020000);
#define O_ADDR(vo, ro, k) unsigned vo; int ro; do { const int r_ = lane >> (3 - (k)), c_ = lane & (7 >> (k));                     \
      vo = (unsigned)(((tm * BM + (wid & 3) * 64 + r_) * N_ + cb0 + ((k) == 0 ? cw : (k) == 1 ? cs : cq)) * 2 + c_ * 16);     \
      ro = HDR + ((k) ? 0 : SB) + (wid & 3) * 8192 + r_ * 128 + (((c_ + ((k) & 2) * 2) ^ (r_ & 7)) << 4);                         \
      asm volatile("" : "+v"(vo), "+v"(ro)); } while (0)
#define O_RD(ro, r_) (*(const u32x4*)(shm_raw + (ro) + (r_) * 128))
#define O_ST(v_, vo, r_, aux) __builtin_amdgcn_raw_buffer_store_b128(v_, rsO, (vo) + (r_) * (N_ * 2), 0, aux)
#define IO_OF() const int io0 = nc * 16 + (lane >> 5) * 448, io1 = nc * 16 + 448 - (lane >> 5) * 448

  if (__builtin_expect(wid < 4, 1)) {
    QRegs q; u32x2 qsv; unsigned c0, c1, tagv, xt0;
    asm volatile("global_load_ushort %0, %2, %4\n\tglobal_load_ushort %1, %3, %4" : "=&v"(c0), "=&v"(c1)
                 : "v"((unsigned)((lane & 15) * 2)), "v"((unsigned)((wid * 4 + (lane >> 4)) * 2)), "s"(cb) : "memory");
    TAG_LOAD(tagv);
    Q_SC(qsv, tm);
    asm volatile("global_load_dword %0, %1, %2" : "=&v"(xt0) : "v"(xtv), "s"(x) : "memory");
    Q_ROWS(q, Q_SO(tm));
    SCHED();
    IO_OF();
    const unsigned gb = (unsigned)((tn * NT + tm) * WB + kc0 * KCB);
    unsigned go0 = qact ? gb + io0 : 0x7fffffffu, go1 = qact ? gb + io1 : 0x7fffffffu;
    asm volatile("" : "+v"(go0), "+v"(go1));
    f32x4 acc[4][7];
#pragma unroll
    for (int m = 0; m < 4; m++)
#pragma unroll
      for (int n = 0; n < 7; n++) { acc[m][n] = (f32x4){0.f, 0.f, 0.f, 0.f}; asm volatile("" : "+v"(acc[m][n])); }
    WAIT_V(11);
    asm volatile("" : "+v"(c0), "+v"(c1)); asm volatile("" :: "v"(ct));
    *cnt = 0;
    *(u32x2*)(lut + tid * 8) = (u32x2){c0 << 16, c1 << 16};
    int fa = FLG + wid * 320 + lane * 4;
    asm volatile("" : "+v"(fa));
    *(unsigned*)(shm_raw + fa + 64) = 0u;
    WAIT_L(0);
    BAR();
    ROUND(q, qsv, XB + kc0 * KCB, 0, 0);
    WAIT_L(0);
    SIGNAL(1u);
    SCHED();
    __builtin_amdgcn_raw_buffer_store_b128(po[0], rsW, go0, 0, AUX_SC1);
    __builtin_amdgcn_raw_buffer_store_b128(po[1], rsW, go0 + 896, 0, AUX_SC1);
    __builtin_amdgcn_raw_buffer_store_b128(po[2], rsW, go1 + 2 * 896, 0, AUX_SC1);
    __builtin_amdgcn_raw_buffer_store_b128(po[3], rsW, go1 + 3 * 896, 0, AUX_SC1);
    SCHED();
    const int fr = lane & 15, fq = lane >> 4;
    const int xo0 = (wid * 64 + fr) * 128 + ((fq ^ (fr >> 1)) << 4), xo1 = xo0 ^ 64;
    const int wo = XB + (fq >> 1) * (2 * KCB) + (fr & 3) * 896 + ((fq ^ (fr >> 1)) & 1) * 448 + (fr >> 2) * 16;
    bf16x8 xa[4], wa[7], xb[4], wb[7];
#define LD(o_) (*(const bf16x8*)(shm + (o_)))
#define MF(m_, n_, xv, w_) do { asm volatile("v_mfma_f32_16x16x32_bf16 %0, %1, %2, %0" : "+v"(acc[m_][n_]) : "v"(w_[n_]), "v"(xv)); SCHED(); } while (0)
#define MF2(m_, n_, x1, w1, x2, w2) do { asm volatile("v_mfma_f32_16x16x32_bf16 %0, %1, %2, %0\n\tv_mfma_f32_16x16x32_bf16 %0, %3, %4, %0" \
      : "+v"(acc[m_][n_]) : "v"(w1[n_]), "v"(x1), "v"(w2[n_]), "v"(x2)); SCHED(); } while (0)
#define MF3(m_, n_, x1, w1, x2, w2, x3, w3) do { asm volatile("v_mfma_f32_16x16x32_bf16 %0, %1, %2, %0\n\tv_mfma_f32_16x16x32_bf16 %0, %3, %4, %0\n\tv_mfma_f32_16x16x32_bf16 %0, %5, %6, %0" \
      : "+v"(acc[m_][n_]) : "v"(w1[n_]), "v"(x1), "v"(w2[n_]), "v"(x2), "v"(w3[n_]), "v"(x3)); SCHED(); } while (0)
#define RX(x_, b_, m_) x_[m_] = LD((b_) + (m_) * 2048)
#define RW(w_, b_, hf, n_) w_[n_] = LD((b_) + (hf) * (4 * KCB) + (n_) * 64)
#define WL(n_) do { SCHED(); __builtin_amdgcn_s_waitcnt(0xC07F | ((n_) << 8)); SCHED(); } while (0)
    O_ADDR(vq, rq, 2);
    int e0 = HDR + wid * 8192 + fr * 128 + (((fq >> 1) ^ (fr & 7)) << 4) + (fq & 1) * 8, e1 = e0 ^ 32, e2 = e0 ^ 64, e3 = e0 ^ 96;
    asm volatile("" : "+v"(e0), "+v"(e1), "+v"(e2), "+v"(e3));
    AWAIT(15u, 6u);
#define STAGE(st) do { int aw_ = wo + (st) * SB; asm volatile("" : "+v"(aw_)); \
      RX(xa, xo0 + (st) * SB, 0); SCHED(); RW(wa, aw_, 0, 0); SCHED(); RX(xb, xo1 + (st) * SB, 0); SCHED(); RW(wb, aw_, 1, 0); SCHED(); \
      RW(wb, aw_, 1, 1); SCHED(); RW(wa, aw_, 0, 1); SCHED(); RW(wa, aw_, 0, 2); SCHED(); RW(wb, aw_, 1, 2); SCHED(); \
      RW(wb, aw_, 1, 3); SCHED(); RW(wa, aw_, 0, 3); SCHED(); \
      MF(0, 0, xa[0], wa); RX(xa, xo0 + (st) * SB, 1); SCHED(); \
      MF(0, 0, xb[0], wb); RX(xb, xo1 + (st) * SB, 1); SCHED(); \
      MF(0, 1, xb[0], wb); RX(xa, xo0 + (st) * SB, 2); SCHED(); \
      MF(0, 1, xa[0], wa); RX(xb, xo1 + (st) * SB, 2); SCHED(); \
      MF(0, 2, xa[0], wa); RX(xa, xo0 + (st) * SB, 3); SCHED(); \
      MF(0, 2, xb[0], wb); RX(xb, xo1 + (st) * SB, 3); SCHED(); \
      MF(0, 3, xb[0], wb); RW(wa, aw_, 0, 4); SCHED(); \
      MF(0, 3, xa[0], wa); RW(wb, aw_, 1, 4); SCHED(); \
      MF(1, 3, xa[1], wa); RW(wb, aw_, 1, 5); SCHED(); \
      MF(1, 3, xb[1], wb); RW(wa, aw_, 0, 5); SCHED(); \
      MF(1, 2, xb[1], wb); RW(wa, aw_, 0, 6); SCHED(); \
      MF(1, 2, xa[1], wa); RW(wb, aw_, 1, 6); SCHED(); \
      MF2(1, 1, xa[1], wa, xb[1], wb); \
      MF2(1, 0, xb[1], wb, xa[1], wa); \
      MF2(2, 0, xa[2], wa, xb[2], wb); \
      MF2(2, 1, xb[2], wb, xa[2], wa); \
      MF2(2, 2, xa[2], wa, xb[2], wb); \
      MF2(2, 3, xb[2], wb, xa[2], wa); \
      MF2(3, 3, xa[3], wa, xb[3], wb); \
      MF2(3, 2, xb[3], wb, xa[3], wa); \
      if ((st) == 0) {                            \
        WAIT_V(0); asm volatile("" : "+v"(tagv) : "v"(xt0)); \
        if (lane == 0) __hip_atomic_store(flags + 256 + tm * 32 + wid, TAG_OF(tagv), RLX_AGENT); } \
      MF2(3, 1, xa[3], wa, xb[3], wb); \
      MF2(3, 0, xb[3], wb, xa[3], wa); \
      MF2(0, 4, xa[0], wa, xb[0], wb); \
      MF2(0, 5, xb[0], wb, xa[0], wa); \
      MF2(0, 6, xa[0], wa, xb[0], wb); \
      MF2(1, 6, xb[1], wb, xa[1], wa); \
      MF2(1, 5, xa[1], wa, xb[1], wb); \
      MF2(1, 4, xb[1], wb, xa[1], wa); \
      MF2(2, 4, xa[2], wa, xb[2], wb); \
      MF2(2, 5, xb[2], wb, xa[2], wa); \
      MF2(2, 6, xa[2], wa, xb[2], wb); \
      MF2(3, 6, xb[3], wb, xa[3], wa); \
      MF2(3, 5, xa[3], wa, xb[3], wb); \
      MF2(3, 4, xb[3], wb, xa[3], wa); \
      } while (0)
    STAGE(0);
    BAR();
    STAGE(1);
    asm volatile(".rept 4\n\ts_nop 0\n\t.endr");
    BAR();
    int aw = wo + STG_OFF - XB, a0 = xo0 + 2 * SB, a1 = xo1 + 2 * SB, so = 2 * SB;
    asm volatile("" : "+v"(aw), "+v"(a0), "+v"(a1));
      RW(wa, aw, 0, 1); SCHED(); RW(wb, aw, 1, 0); SCHED(); RW(wa, aw, 0, 0); SCHED(); RW(wb, aw, 1, 1); SCHED();
      RX(xa, a0, 0); SCHED(); RX(xb, a1, 0); SCHED(); RW(wa, aw, 0, 2); SCHED(); RW(wb, aw, 1, 2); SCHED();
      RW(wb, aw, 1, 3); SCHED(); RW(wa, aw, 0, 3); SCHED(); RX(xa, a0, 1); SCHED(); RX(xb, a1, 1); SCHED();
      RX(xa, a0, 2); SCHED(); RX(xb, a1, 2); SCHED();
    WL(5);
#define HALF_A() do { \
      MF(0, 0, xa[0], wa); RX(xa, a0, 3); SCHED(); \
      MF(0, 0, xb[0], wb); \
      MF(0, 1, xb[0], wb); RX(xb, a1, 3); SCHED(); \
      MF(0, 1, xa[0], wa); \
      MF(0, 2, xa[0], wa); RW(wa, aw, 0, 4); SCHED(); \
      MF(0, 2, xb[0], wb); WL(4); SCHED(); \
      MF(0, 3, xb[0], wb); RW(wb, aw, 1, 4); SCHED(); \
      MF(0, 3, xa[0], wa); \
      MF(1, 3, xa[1], wa); RW(wb, aw, 1, 5); SCHED(); \
      MF(1, 3, xb[1], wb); \
      MF(1, 2, xb[1], wb); RW(wa, aw, 0, 5); SCHED(); \
      MF(1, 2, xa[1], wa); \
      MF(1, 1, xa[1], wa); RW(wa, aw, 0, 6); SCHED(); \
      MF(1, 1, xb[1], wb); \
      MF(1, 0, xb[1], wb); RW(wb, aw, 1, 6); SCHED(); \
      MF(1, 0, xa[1], wa); so = so == 2 * SB ? 0 : so + SB; asm volatile("" : "+s"(so)); SCHED(); \
      MF(2, 0, xa[2], wa); WL(3); aw = wo + so; a0 = xo0 + so; asm volatile("" : "+v"(aw), "+v"(a0)); SCHED(); \
      MF(2, 0, xb[2], wb); a1 = xo1 + so; asm volatile("" : "+v"(a1)); SCHED(); \
      MF2(2, 1, xb[2], wb, xa[2], wa); \
      MF2(2, 2, xa[2], wa, xb[2], wb); \
      MF2(2, 3, xb[2], wb, xa[2], wa); \
      MF2(3, 3, xa[3], wa, xb[3], wb); \
      MF(3, 2, xb[3], wb); WL(0); SCHED(); \
      MF(3, 2, xa[3], wa); BAR(); SCHED(); \
      } while (0)
#pragma unroll 1
    for (int t = NT - 5; t;) {
      HALF_A();
      MF(3, 1, xa[3], wa); RW(wa, aw, 0, 1); SCHED();
      MF(3, 1, xb[3], wb);
      MF(3, 0, xb[3], wb); RW(wb, aw, 1, 0); SCHED();
      MF(3, 0, xa[3], wa);
      MF(0, 4, xa[0], wa); RW(wa, aw, 0, 0); SCHED();
      MF(0, 4, xb[0], wb);
      MF(0, 5, xb[0], wb); RW(wb, aw, 1, 1); SCHED();
      MF(0, 5, xa[0], wa);
      MF(0, 6, xa[0], wa); RX(xa, a0, 0); SCHED();
      MF(0, 6, xb[0], wb);
      MF(1, 6, xb[1], wb); RX(xb, a1, 0); SCHED();
      MF(1, 6, xa[1], wa);
      MF(1, 5, xa[1], wa); RW(wa, aw, 0, 2); SCHED();
      MF(1, 5, xb[1], wb);
      MF(1, 4, xb[1], wb); RW(wb, aw, 1, 2); SCHED();
      MF(1, 4, xa[1], wa);
      MF(2, 4, xa[2], wa); RW(wb, aw, 1, 3); SCHED();
      MF(2, 4, xb[2], wb);
      MF(2, 5, xb[2], wb); RW(wa, aw, 0, 3); SCHED();
      MF(2, 5, xa[2], wa);
      MF(2, 6, xa[2], wa); RX(xa, a0, 1); SCHED();
      MF(2, 6, xb[2], wb);
      MF(3, 6, xb[3], wb); RX(xb, a1, 1); SCHED();
      MF(3, 6, xa[3], wa); t--; asm volatile("" : "+s"(t)); SCHED();
      MF(3, 5, xa[3], wa); RX(xa, a0, 2); SCHED();
      MF(3, 5, xb[3], wb); WL(4); SCHED();
      MF(3, 4, xb[3], wb); RX(xb, a1, 2); SCHED();
      MF(3, 4, xa[3], wa);
    }
    HALF_A();
    SCHED();
#define CV(m_, j_) pk[m_][j_] = pack_bf16(acc[m_][(j_) / 2][((j_) & 1) * 2], acc[m_][(j_) / 2][((j_) & 1) * 2 + 1])
#define W64(m_, n_) *(u32x2*)(shm_raw + (((n_) & 3) == 0 ? e0 : ((n_) & 3) == 1 ? e1 : ((n_) & 3) == 2 ? e2 : e3) + ((n_) < 4 ? SB : 0) + (m_) * 2048) = (u32x2){pk[m_][2 * (n_)], pk[m_][2 * (n_) + 1]}
#define W32(m_, n_, h_) *(unsigned*)(shm_raw + (((n_) & 3) == 0 ? e0 : ((n_) & 3) == 1 ? e1 : ((n_) & 3) == 2 ? e2 : e3) + ((n_) < 4 ? SB : 0) + (m_) * 2048 + 4 * (h_)) = pk[m_][2 * (n_) + (h_)]
#define WA(m_, n_) W32(m_, n_, 0)
#define WB_(m_, n_) W32(m_, n_, 1)
#define FLAG(u_) do { asm volatile("" ::: "memory"); *(int*)(shm_raw + fa + 4 * (u_)) = fa; asm volatile("" ::: "memory"); } while (0)
    {
      bf16x8 xc[4], xd[4], xe[4], xf[4], wc[7], wd[7], we[7], wf[7], wg[7], wh[7];
      unsigned pk[4][14];
      u32x4 ov[2];
      MF(3, 1, xa[3], wa); RX(xc, a0, 0); SCHED();
      MF(3, 1, xb[3], wb); RW(wc, aw, 0, 0); SCHED();
      MF(3, 0, xb[3], wb); RX(xd, a1, 0); SCHED();
      MF(3, 0, xa[3], wa); RW(wd, aw, 1, 0); SCHED();
      MF(0, 4, xa[0], wa); RW(wd, aw, 1, 1); SCHED();
      MF(0, 4, xb[0], wb); RW(wc, aw, 0, 1); SCHED();
      MF(0, 5, xb[0], wb); RW(wc, aw, 0, 2); SCHED();
      MF(0, 5, xa[0], wa); RW(wd, aw, 1, 2); SCHED();
      MF(0, 6, xa[0], wa); RW(wd, aw, 1, 3); SCHED();
      MF(0, 6, xb[0], wb); RW(wc, aw, 0, 3); SCHED();
      MF2(1, 6, xb[1], wb, xa[1], wa); RX(xc, a0, 1); SCHED();
      BAR();
      SCHED();
      MF(1, 5, xa[1], wa); RX(xe, xo0, 0); SCHED();
      MF(1, 5, xb[1], wb); RW(we, aw + WB, 0, 0); SCHED();
      MF(1, 4, xb[1], wb); RX(xf, xo1, 0); SCHED();
      MF(1, 4, xa[1], wa); RW(wf, aw + WB, 1, 0); WL(5); SCHED();
      MF(0, 0, xc[0], wc); RW(wf, aw + WB, 1, 1); SCHED();
      MF(0, 0, xd[0], wd); RW(we, aw + WB, 0, 1); SCHED();
      MF(0, 1, xd[0], wd); RW(we, aw + WB, 0, 2); SCHED();
      MF(0, 1, xc[0], wc); RW(wf, aw + WB, 1, 2); SCHED();
      MF(0, 2, xc[0], wc); RW(wf, aw + WB, 1, 3); RW(we, aw + WB, 0, 3); SCHED();
      MF(0, 2, xd[0], wd); RX(xd, a1, 1); RX(xe, xo0, 1); SCHED();
      MF(0, 3, xd[0], wd); RX(xf, xo1, 1); RX(xc, a0, 2); SCHED();
      MF(0, 3, xc[0], wc); RX(xd, a1, 2); WL(8); SCHED();
      MF(0, 0, xe[0], we); RX(xe, xo0, 2); SCHED();
      MF(0, 0, xf[0], wf);
      MF2(0, 1, xf[0], wf, xe[0], we); WL(2); SCHED();
      MF(0, 2, xe[0], we); CV(0, 0); CV(0, 1); SCHED();
      MF(0, 2, xf[0], wf); W64(0, 0); SCHED();
      MF(0, 3, xf[0], wf); CV(0, 2); CV(0, 3); SCHED();
      MF(0, 3, xe[0], we); W64(0, 1); SCHED();
      MF(1, 0, xc[1], wc); CV(0, 4); CV(0, 5); SCHED();
      MF(1, 0, xd[1], wd); W64(0, 2); SCHED();
      MF(1, 0, xe[1], we); CV(0, 6); CV(0, 7); SCHED();
      MF(1, 0, xf[1], wf); W64(0, 3); SCHED();
      MF(1, 1, xf[1], wf); FLAG(1); SCHED();
      MF(1, 1, xe[1], we); RX(xf, xo1, 2); RX(xc, a0, 3); SCHED();
      MF(1, 1, xd[1], wd); CV(1, 0); CV(1, 1); SCHED();
      MF(1, 1, xc[1], wc); W64(1, 0); SCHED();
      MF(1, 2, xc[1], wc); RX(xd, a1, 3); RX(xe, xo0, 3); SCHED();
      MF(1, 2, xd[1], wd); RX(xf, xo1, 3); SCHED();
      MF(1, 2, xe[1], we); CV(1, 2); CV(1, 3); SCHED();
      MF(1, 2, xf[1], wf); W64(1, 1); SCHED();
      MF(1, 3, xf[1], wf); RW(wc, aw, 0, 4); SCHED();
      MF2(1, 3, xe[1], we, xd[1], wd); CV(1, 4); CV(1, 5); SCHED();
      MF(1, 3, xc[1], wc); W64(1, 2); SCHED();
      MF(2, 0, xc[2], wc); RW(wd, aw, 1, 4); WL(4); SCHED();
      MF2(2, 0, xd[2], wd, xe[2], we); CV(1, 6); CV(1, 7); SCHED();
      MF(2, 0, xf[2], wf); W64(1, 3); SCHED();
      MF(2, 1, xf[2], wf); FLAG(2); SCHED();
      MF(2, 1, xe[2], we); RW(we, aw + WB, 0, 4); SCHED();
      MF(2, 1, xd[2], wd); CV(2, 0); CV(2, 1); SCHED();
      MF(2, 1, xc[2], wc); W64(2, 0); SCHED();
      MF(2, 2, xc[2], wc); RW(wf, aw + WB, 1, 4); SCHED();
      MF2(2, 2, xd[2], wd, xe[2], we); CV(2, 2); CV(2, 3); SCHED();
      MF(2, 2, xf[2], wf); W64(2, 1); SCHED();
      MF(2, 3, xf[2], wf); RW(wf, aw + WB, 1, 5); SCHED();
      MF2(2, 3, xe[2], we, xd[2], wd); CV(2, 4); CV(2, 5); SCHED();
      MF(2, 3, xc[2], wc); W64(2, 2); SCHED();
      MF(3, 0, xc[3], wc); RW(we, aw + WB, 0, 5); RW(wd, aw, 1, 5); SCHED();
      MF(3, 0, xd[3], wd); RW(wc, aw, 0, 5); SCHED();
      MF(3, 0, xe[3], we); CV(2, 6); CV(2, 7); SCHED();
      MF(3, 0, xf[3], wf); W64(2, 3); SCHED();
      MF(3, 1, xf[3], wf); FLAG(3); SCHED();
      MF(3, 1, xe[3], we); RW(wg, wo + SB, 0, 4); RW(wh, wo + SB, 1, 4); SCHED();
      MF(3, 1, xd[3], wd); CV(3, 0); CV(3, 1); SCHED();
      MF(3, 1, xc[3], wc); W64(3, 0); SCHED();
      MF(3, 2, xc[3], wc); RW(wg, wo + SB, 0, 5); RW(wh, wo + SB, 1, 5); SCHED();
      MF2(3, 2, xd[3], wd, xe[3], we); CV(3, 2); CV(3, 3); SCHED();
      MF(3, 2, xf[3], wf); W64(3, 1); SCHED();
      MF2(3, 3, xf[3], wf, xe[3], we); WL(1); SCHED();
      MF(3, 3, xd[3], wd); CV(3, 4); CV(3, 5); SCHED();
      MF(3, 3, xc[3], wc); W64(3, 2); SCHED();
      MF(0, 4, xc[0], wc); RW(wc, aw, 0, 6); SCHED();
      MF(0, 4, xd[0], wd); RW(wd, aw, 1, 6); SCHED();
      MF(0, 4, xe[0], we); CV(3, 6); CV(3, 7); SCHED();
      MF(0, 4, xf[0], wf); W64(3, 3); SCHED();
      MF(0, 5, xf[0], wf); FLAG(4); SCHED();
      MF(0, 5, xe[0], we); RW(we, aw + WB, 0, 6); RW(wf, aw + WB, 1, 6); SCHED();
      MF(0, 5, xd[0], wd); CV(0, 8); CV(0, 9); SCHED();
      MF(0, 5, xc[0], wc); WA(0, 4); SCHED();
      MF(1, 4, xc[1], wc); WB_(0, 4); SCHED();
      MF(1, 4, xd[1], wd); RW(wg, wo + SB, 0, 6); RW(wh, wo + SB, 1, 6); SCHED();
      MF(1, 4, xe[1], we); CV(0, 10); CV(0, 11); SCHED();
      MF(1, 4, xf[1], wf); WA(0, 5); SCHED();
      MF(1, 5, xf[1], wf); WB_(0, 5); SCHED();
      MF(1, 5, xe[1], we); FLAG(5); SCHED();
      MF(1, 5, xd[1], wd); CV(1, 8); CV(1, 9); SCHED();
      MF(1, 5, xc[1], wc); WA(1, 4); SCHED();
      MF(2, 4, xa[2], wg); WB_(1, 4); SCHED();
      MF2(2, 4, xb[2], wh, xc[2], wc); CV(1, 10); CV(1, 11); SCHED();
      MF(2, 4, xd[2], wd); WA(1, 5); SCHED();
      MF(2, 4, xe[2], we); WB_(1, 5); SCHED();
      MF(2, 4, xf[2], wf); FLAG(6); SCHED();
      MF3(2, 5, xf[2], wf, xe[2], we, xd[2], wd); CV(2, 8); CV(2, 9); SCHED();
      MF(2, 5, xc[2], wc); WA(2, 4); SCHED();
      MF(2, 5, xa[2], wg); WB_(2, 4); SCHED();
      MF(2, 5, xb[2], wh);
      MF3(3, 4, xb[3], wh, xa[3], wg, xc[3], wc); CV(2, 10); CV(2, 11); SCHED();
      MF(3, 4, xd[3], wd); WA(2, 5); SCHED();
      MF(3, 4, xe[3], we); WB_(2, 5); SCHED();
      MF(3, 4, xf[3], wf); FLAG(7); SCHED();
      MF3(3, 5, xf[3], wf, xe[3], we, xd[3], wd); CV(3, 8); CV(3, 9); SCHED();
      MF(3, 5, xc[3], wc); WA(3, 4); SCHED();
      MF(3, 5, xb[3], wh); WB_(3, 4); SCHED();
      MF(3, 5, xa[3], wg); WL(15); SCHED();
      MF3(0, 6, xc[0], wc, xd[0], wd, xe[0], we); CV(3, 10); CV(3, 11); SCHED();
      MF(0, 6, xf[0], wf); WA(3, 5); SCHED();
      MF(1, 6, xf[1], wf); WB_(3, 5); SCHED();
      MF(1, 6, xe[1], we); FLAG(8); SCHED();
      MF(1, 6, xd[1], wd); CV(0, 12); CV(0, 13); SCHED();
      MF(1, 6, xc[1], wc); WA(0, 6); SCHED();
      MF(2, 6, xa[2], wg); WB_(0, 6); SCHED();
      MF2(2, 6, xb[2], wh, xc[2], wc); CV(1, 12); CV(1, 13); SCHED();
      MF(2, 6, xd[2], wd); WA(1, 6); SCHED();
      MF(2, 6, xe[2], we); WB_(1, 6); SCHED();
      MF(2, 6, xf[2], wf); ov[0] = O_RD(rq, 0); SCHED();
      MF3(3, 6, xf[3], wf, xe[3], we, xd[3], wd); CV(2, 12); CV(2, 13); SCHED();
      MF(3, 6, xc[3], wc); WA(2, 6); SCHED();
      MF(3, 6, xa[3], wg); WB_(2, 6); SCHED();
      MF(3, 6, xb[3], wh);
      O_ST(ov[0], vq, 0, 0); SCHED();
      asm volatile("s_nop 6");
      SCHED();
      CV(3, 12); CV(3, 13); W64(3, 6); ov[1] = O_RD(rq, 32); SCHED();
      O_ST(ov[1], vq, 32, 0);
    }
  } else {
    IO_OF();
    const unsigned long long ord = order_of(tm, tn);
#define KS(s_) ((int)(ord >> (4 * (s_))) & 15)
    const int h = wid & 1;
    unsigned xv[16];
#pragma unroll
    for (int i = 0; i < 16; i++) xv[i] = (unsigned)((8 * i + (lane >> 3)) * (K_ * 2) + (((lane & 7) ^ ((i & 1) * 4 + (lane >> 4))) << 4));
    const char* xbase = (const char*)(x + (size_t)(tm * BM + h * 128) * K_);
    const unsigned ldsx = (unsigned)(size_t)(shm) + h * 16384;
#define X8(o, l_, b_)                                                                                                             \
      asm volatile("s_mov_b32 m0, %8\n\ts_nop 0\n\tglobal_load_lds_dwordx4 %0, %9\n\t"                                            \
                   "s_add_u32 m0, m0, 0x400\n\ts_nop 0\n\tglobal_load_lds_dwordx4 %1, %9\n\t"                                     \
                   "s_add_u32 m0, m0, 0x400\n\ts_nop 0\n\tglobal_load_lds_dwordx4 %2, %9\n\t"                                     \
                   "s_add_u32 m0, m0, 0x400\n\ts_nop 0\n\tglobal_load_lds_dwordx4 %3, %9\n\t"                                     \
                   "s_add_u32 m0, m0, 0x400\n\ts_nop 0\n\tglobal_load_lds_dwordx4 %4, %9\n\t"                                     \
                   "s_add_u32 m0, m0, 0x400\n\ts_nop 0\n\tglobal_load_lds_dwordx4 %5, %9\n\t"                                     \
                   "s_add_u32 m0, m0, 0x400\n\ts_nop 0\n\tglobal_load_lds_dwordx4 %6, %9\n\t"                                     \
                   "s_add_u32 m0, m0, 0x400\n\ts_nop 0\n\tglobal_load_lds_dwordx4 %7, %9"                                         \
                   :: "v"(xv[o]), "v"(xv[o + 1]), "v"(xv[o + 2]), "v"(xv[o + 3]), "v"(xv[o + 4]), "v"(xv[o + 5]), "v"(xv[o + 6]), "v"(xv[o + 7]), \
                      "s"(l_), "s"(b_) : "memory", "scc")
#define ISSUE_X(s, slot) do { const char* b_ = xbase + KS(s) * 128; const unsigned l_ = ldsx + (slot) * SB;                       \
      X8(0, l_, b_); X8(8, l_ + 8192, b_); } while (0)
    const int fj = (int)(ord >> (4 * min(lane >> 2, 12) + 12));
    const unsigned fvo = (unsigned)(1024 + (fj & 7) * 128 + (fj & 8) * 2 + (lane & 3) * 4);
    O_ADDR(vw, rw, 0); O_ADDR(vs, rs, 1);
    unsigned fp = (unsigned)(size_t)shm_raw + FLG + (wid & 3) * 320 + 256;
    asm volatile("" : "+v"(fp));
    unsigned va[12];
#pragma unroll
    for (int i = 0; i < 12; i++) { va[i] = i < 8 ? vw + i * 8 * (N_ * 2) : vs + (i - 8) * 16 * (N_ * 2); asm volatile("" : "+v"(va[i])); }
  if (wid < 6) {
    QRegs qa, q; u32x2 sa, sb;
    Q_SC(sa, k1);
    Q_SC(sb, k2);
    BAR();
    ISSUE_X(0, 0);
    SIGNAL(0x1000u);
    Q_ROWS(qa, Q_SO(k1));
    WAIT_V(8);
    SIGNAL(1u);
    { const char* b_ = xbase + k1 * 128; const unsigned l_ = ldsx + SB; X8(0, l_, b_); X8(8, l_ + 8192, b_); }
    SIGNAL(16u);
    asm volatile("" :: "v"(ct));
    Q_ROWS(q, Q_SO(k2));
    ROUND(qa, sa, SB + XB + kc0 * KCB, 24, 1);
    WAIT_V(8);
    WAIT_L(0);
    BAR();
    X8(0, ldsx + 2 * SB, xbase + k2 * 128);
    ROUND(q, sb, STG_OFF + kc0 * KCB, 8, 1);
    X8(8, ldsx + 2 * SB + 8192, xbase + k2 * 128);
    WAIT_L(0);
    unsigned long long o = ord >> 12; unsigned l_ = ldsx;
#pragma unroll 1
    for (int t = 0; t < NT - 3; t++) {
      const char* b_ = xbase + ((unsigned)o & 15) * 128; unsigned l2 = l_ + 8192;
      asm volatile("" : "+s"(b_), "+s"(l_), "+s"(l2));
      if (t) { WAIT_V(16); BAR(); }
      X8(0, l_, b_); X8(8, l2, b_);
      o >>= 4; l_ = l_ == ldsx + 2 * SB ? ldsx : l_ + SB;
    }
    WAIT_V(16); BAR();
    WAIT_V(0); BAR();
  } else {
    QRegs q, qa; u32x2 sa, sb; unsigned tagv;
    Q_SC(sa, k1); Q_SC(sb, k2); TAG_LOAD(tagv);
    BAR();
    AWAIT(0xf000u, 0x2000u);
    Q_ROWS(qa, Q_SO(k1));
    ROUND(qa, sa, SB + XB + kc0 * KCB, 0, 1);
    AWAIT(0xf0u, 32u);
    Q_ROWS(q, Q_SO(k2));
    asm volatile("" :: "v"(ct));
    WAIT_L(0);
    asm volatile("" : "+v"(tagv));
    const unsigned tag = TAG_OF(tagv);
    unsigned fv = tagv;
    const unsigned fvo6 = wid == 6 ? fvo : 0u;
    BAR();
    asm volatile("global_load_dword %0, %1, %2 sc1" : "+v"(fv) : "v"(fvo6), "s"(flags) : "memory");
    ROUND(q, sb, STG_OFF + kc0 * KCB, 1, 1);
    WAIT_L(0);
    const char* wbase = ws + (size_t)tn * NT * WB;
    const unsigned wv = (unsigned)(lane * 16), wv2 = wv + 4096, wv3 = wv + 8192, wv4 = wv + 12288;
    const unsigned ldsw = (unsigned)(size_t)(shm) + XB;
#define W4(v_, l_, b_) asm volatile("s_mov_b32 m0, %1\n\ts_nop 0\n\t"                                                             \
                   "global_load_lds_dwordx4 %0, %2 sc1\n\tglobal_load_lds_dwordx4 %0, %2 offset:1024 sc1\n\t"                     \
                   "global_load_lds_dwordx4 %0, %2 offset:2048 sc1\n\tglobal_load_lds_dwordx4 %0, %2 offset:3072 sc1" :: "v"(v_), "s"(l_), "s"(b_) : "memory")
#define W2(v_, l_, b_) asm volatile("s_mov_b32 m0, %1\n\ts_nop 0\n\t"                                                             \
                   "global_load_lds_dwordx4 %0, %2 sc1\n\tglobal_load_lds_dwordx4 %0, %2 offset:1024 sc1" :: "v"(v_), "s"(l_), "s"(b_) : "memory")
#define W1(v_, o_, l_, b_) asm volatile("s_mov_b32 m0, %1\n\ts_nop 0\n\tglobal_load_lds_dwordx4 %0, %2 offset:%3 sc1" :: "v"(v_), "s"(l_), "s"(b_), "n"(o_) : "memory")
   if (__builtin_expect(wid == 6, 1)) {
    asm volatile(
      "s_lshr_b64 s[64:65], %[ord], 12\n\ts_mov_b32 s66, 0\n\ts_mov_b32 s67, 3\n\ts_mov_b32 s69, %[l0]\n\t"
      "s_mov_b32 s70, 0\n\ts_mov_b32 s71, 0\n\ts_mov_b32 m0, s69\n\ts_and_b32 s60, s64, 15\n\ts_mulk_i32 s60, 0x3800\n\t"
      "s_add_u32 s60, s60, %[blo]\n\ts_addc_u32 s61, %[bhi], 0\n\ts_waitcnt vmcnt(0)\n\t"
      "v_cmp_ne_u32_e64 s[62:63], %[fv], %[tag]\n\ts_bitset1_b32 s63, 20\n\ts_ff1_i32_b64 s68, s[62:63]\n\t"
      "s_lshr_b32 s68, s68, 2\n\ts_add_i32 s68, s68, 2\n\ts_mov_b64 vcc, 0\n\ts_cmp_gt_i32 s67, s68\n\t"
      "s_cbranch_scc1 .Lw%=\n\ts_branch .Lf%=\n\t"
      ".La%=:\n\ts_waitcnt vmcnt(14)\n\t"
      ".Lb%=:\n\ts_waitcnt vmcnt(15)\n\t"
      "s_barrier\n\ts_cbranch_scc1 .Ls%=\n\ts_sleep 5\n\t"
      ".Lf%=:\n\tglobal_load_lds_dwordx4 %[w0], s[60:61] sc1\n\t"
      "global_load_lds_dwordx4 %[w0], s[60:61] offset:1024 sc1\n\t"
      "global_load_lds_dwordx4 %[w0], s[60:61] offset:2048 sc1\n\t"
      "global_load_lds_dwordx4 %[w0], s[60:61] offset:3072 sc1\n\ts_cbranch_vccz .Lg%=\n\ts_waitcnt vmcnt(14)\n\t"
      "v_cmp_ne_u32_e64 s[62:63], %[fv], %[tag]\n\ts_bitset1_b32 s63, 20\n\ts_ff1_i32_b64 s68, s[62:63]\n\t"
      "s_lshr_b32 s68, s68, 2\n\ts_add_i32 s68, s68, 2\n\t"
      ".Lg%=:\n\ts_add_i32 s62, s66, 5\n\ts_min_i32 s62, s62, 15\n\ts_cmp_lt_i32 s68, s62\n\ts_cselect_b64 vcc, -1, 0\n\t"
      "s_cbranch_scc0 .Lh%=\n\tglobal_load_dword %[fv], %[fvo], %[fl] sc1\n\t"
      ".Lh%=:\n\ts_add_u32 m0, m0, 0x1000\n\ts_nop 0\n\tglobal_load_lds_dwordx4 %[w1], s[60:61] sc1\n\t"
      "global_load_lds_dwordx4 %[w1], s[60:61] offset:1024 sc1\n\t"
      "global_load_lds_dwordx4 %[w1], s[60:61] offset:2048 sc1\n\t"
      "global_load_lds_dwordx4 %[w1], s[60:61] offset:3072 sc1\n\ts_add_u32 m0, m0, 0x1000\n\ts_nop 0\n\t"
      "global_load_lds_dwordx4 %[w2], s[60:61] sc1\n\tglobal_load_lds_dwordx4 %[w2], s[60:61] offset:1024 sc1\n\t"
      "global_load_lds_dwordx4 %[w2], s[60:61] offset:2048 sc1\n\t"
      "global_load_lds_dwordx4 %[w2], s[60:61] offset:3072 sc1\n\ts_add_u32 m0, m0, 0x1000\n\ts_nop 0\n\t"
      "global_load_lds_dwordx4 %[w3], s[60:61] sc1\n\tglobal_load_lds_dwordx4 %[w3], s[60:61] offset:1024 sc1\n\t"
      "s_add_i32 s67, s67, 1\n\ts_lshr_b64 s[64:65], s[64:65], 4\n\ts_and_b32 s60, s64, 15\n\ts_mulk_i32 s60, 0x3800\n\t"
      "s_add_u32 s60, s60, %[blo]\n\ts_addc_u32 s61, %[bhi], 0\n\ts_add_i32 s69, s69, 0xb800\n\t"
      "s_cmp_gt_u32 s69, %[lmax]\n\ts_cselect_b32 s69, %[l0], s69\n\ts_mov_b32 m0, s69\n\ts_add_i32 s66, s66, 1\n\t"
      "s_mov_b32 s70, 1\n\ts_cmp_gt_i32 s67, 14\n\ts_cbranch_scc1 .Lx%=\n\ts_cmp_gt_i32 s67, s68\n\t"
      "s_cbranch_vccz .La%=\n\ts_branch .Lb%=\n\t"
      ".Ls%=:\n\ts_cmp_lg_u32 s70, 0\n\ts_cbranch_scc1 .Lt%=\n\ts_waitcnt vmcnt(0)\n\t"
      ".Lt%=:\n\ts_waitcnt vmcnt(10)\n\tv_cmp_ne_u32_e64 s[62:63], %[fv], %[tag]\n\ts_bitset1_b32 s63, 20\n\t"
      "s_ff1_i32_b64 s68, s[62:63]\n\ts_lshr_b32 s68, s68, 2\n\ts_add_i32 s68, s68, 2\n\ts_add_i32 s62, s66, 2\n\t"
      "s_cmp_gt_i32 s67, s62\n\ts_cbranch_scc1 .Lw%=\n\t"
      ".Lu%=:\n\ts_cmp_le_i32 s67, s68\n\ts_cbranch_scc1 .Lv%=\n\ts_add_i32 s71, s71, 1\n\ts_mov_b32 s68, 16\n\t"
      "s_cmp_gt_u32 s71, %[smax]\n\ts_cbranch_scc1 .Lv%=\n\tglobal_load_dword %[fv], %[fvo], %[fl] sc1\n\t"
      "s_waitcnt vmcnt(0)\n\tv_cmp_ne_u32_e64 s[62:63], %[fv], %[tag]\n\ts_bitset1_b32 s63, 20\n\t"
      "s_ff1_i32_b64 s68, s[62:63]\n\ts_lshr_b32 s68, s68, 2\n\ts_add_i32 s68, s68, 2\n\ts_branch .Lu%=\n\t"
      ".Lv%=:\n\tglobal_load_lds_dwordx4 %[w0], s[60:61] sc1\n\t"
      "global_load_lds_dwordx4 %[w0], s[60:61] offset:1024 sc1\n\t"
      "global_load_lds_dwordx4 %[w0], s[60:61] offset:2048 sc1\n\t"
      "global_load_lds_dwordx4 %[w0], s[60:61] offset:3072 sc1\n\ts_add_u32 m0, m0, 0x1000\n\ts_nop 0\n\t"
      "global_load_lds_dwordx4 %[w1], s[60:61] sc1\n\tglobal_load_lds_dwordx4 %[w1], s[60:61] offset:1024 sc1\n\t"
      "global_load_lds_dwordx4 %[w1], s[60:61] offset:2048 sc1\n\t"
      "global_load_lds_dwordx4 %[w1], s[60:61] offset:3072 sc1\n\ts_add_u32 m0, m0, 0x1000\n\ts_nop 0\n\t"
      "global_load_lds_dwordx4 %[w2], s[60:61] sc1\n\tglobal_load_lds_dwordx4 %[w2], s[60:61] offset:1024 sc1\n\t"
      "global_load_lds_dwordx4 %[w2], s[60:61] offset:2048 sc1\n\t"
      "global_load_lds_dwordx4 %[w2], s[60:61] offset:3072 sc1\n\ts_add_u32 m0, m0, 0x1000\n\ts_nop 0\n\t"
      "global_load_lds_dwordx4 %[w3], s[60:61] sc1\n\tglobal_load_lds_dwordx4 %[w3], s[60:61] offset:1024 sc1\n\t"
      "s_add_i32 s67, s67, 1\n\ts_lshr_b64 s[64:65], s[64:65], 4\n\ts_and_b32 s60, s64, 15\n\ts_mulk_i32 s60, 0x3800\n\t"
      "s_add_u32 s60, s60, %[blo]\n\ts_addc_u32 s61, %[bhi], 0\n\ts_add_i32 s69, s69, 0xb800\n\t"
      "s_cmp_gt_u32 s69, %[lmax]\n\ts_cselect_b32 s69, %[l0], s69\n\ts_mov_b32 m0, s69\n\ts_cmp_gt_i32 s67, 14\n\t"
      "s_cbranch_scc1 .Ly%=\n\t"
      ".Lw%=:\n\ts_mov_b64 vcc, 0\n\ts_cmp_le_i32 s67, s68\n\ts_cbranch_scc1 .Lf%=\n\t"
      "global_load_dword %[fv], %[fvo], %[fl] sc1\n\ts_add_i32 s66, s66, 1\n\ts_mov_b32 s70, 0\n\ts_cmp_eq_u32 s70, 0\n\t"
      "s_waitcnt vmcnt(1)\n\ts_branch .Lb%=\n\t"
      ".Lx%=:\n\ts_waitcnt vmcnt(14)\n\t"
      "s_barrier\n\t"
      ".Ly%=:\n\ts_add_i32 s66, s66, 1\n\ts_cmp_lt_u32 s66, 14\n\ts_waitcnt vmcnt(0)\n\t"
      "s_barrier\n\ts_cbranch_scc1 .Ly%=\n\t"
      : [fv] "+v"(fv)
      : [tag] "v"(tag), [fvo] "v"(fvo), [w0] "v"(wv), [w1] "v"(wv2), [w2] "v"(wv3), [w3] "v"(wv4), [fl] "s"(flags), [ord] "s"(ord),
        [blo] "s"((unsigned)(size_t)wbase), [bhi] "s"((unsigned)((size_t)wbase >> 32)), [l0] "s"(ldsw), [lmax] "s"(ldsw + 2 * SB), [smax] "s"(SPIN_MAX)
      : "memory", "scc", "vcc", "s60", "s61", "s62", "s63", "s64", "s65", "s66", "s67", "s68", "s69", "s70", "s71");
   } else {
#define SET_RDY(f_) do { const unsigned long long b_ = __ballot((f_) != tag);                                                     \
      rdy = 2 + ((int)__builtin_ctzll(b_ | (1ull << 52)) >> 2); } while (0)
#define POLL_BLOCK() do { unsigned f_; asm volatile("global_load_dword %0, %1, %2 sc1\n\ts_waitcnt vmcnt(0)" : "=&v"(f_) : "v"(fvo), "s"(flags) : "memory"); \
      SET_RDY(f_); } while (0)
    const char* b7 = wbase + KS(NT - 1) * WB; const unsigned l7 = (unsigned)(size_t)(shm) + STG_OFF;
    BAR();
#pragma unroll 1
    for (int j = 0; j < 2; j++) PUBLISH(STG_OFF + 2 * j * KCB, k2, j);
    BAR();
#pragma unroll 1
    for (int j = 2; j < 4; j++) PUBLISH(STG_OFF + 2 * j * KCB, k2, j);
    BAR();
    WAIT_V(0);
    if (lane < 4) __hip_atomic_store(flags + 256 + tm * 32 + 4 + lane, tag, RLX_AGENT);
    BAR();
    BAR();
    BAR();
    BAR();
    asm volatile("global_load_dword %0, %1, %2 sc1" : "+v"(fv) : "v"(fvo), "s"(flags) : "memory");
    BAR();
    WAIT_V(0);
    asm volatile("" : "+v"(fv));
    int rdy; SET_RDY(fv);
    const bool ok = rdy == NT - 1;
    if (ok && tm == 0 && lane == 0) __hip_atomic_store(flags, tag, RLX_AGENT);
    if (ok) { __builtin_amdgcn_s_sleep(6); W1(wv, 0, l7 + 0, b7); W1(wv, 1024, l7 + 0, b7); W1(wv, 2048, l7 + 0, b7); W1(wv, 3072, l7 + 0, b7); }
    BAR();
    if (ok) { __builtin_amdgcn_s_sleep(6); W1(wv2, 0, l7 + 4096, b7); W1(wv2, 1024, l7 + 4096, b7); W1(wv2, 2048, l7 + 4096, b7); W1(wv2, 3072, l7 + 4096, b7); }
    BAR();
    if (ok) { __builtin_amdgcn_s_sleep(6); W1(wv3, 0, l7 + 8192, b7); W1(wv3, 1024, l7 + 8192, b7); W1(wv3, 2048, l7 + 8192, b7); W1(wv3, 3072, l7 + 8192, b7); }
    BAR();
    if (ok) { __builtin_amdgcn_s_sleep(6); W1(wv4, 0, l7 + 12288, b7); W1(wv4, 1024, l7 + 12288, b7); }
    BAR();
    if (__builtin_expect(!ok, 0)) {
      unsigned spins = 0;
#pragma unroll 1
      while (rdy < NT - 1) { if (++spins > SPIN_MAX) break; POLL_BLOCK(); }
      W4(wv, l7, b7); W4(wv2, l7 + 4096, b7); W4(wv3, l7 + 8192, b7); W2(wv4, l7 + 12288, b7);
      if (tm == 0 && lane == 0) __hip_atomic_store(flags, tag, RLX_AGENT);
    }
    BAR();
    WAIT_V(0);
    BAR();
   }
  }
#define UNIT(u_, a0_, o0_, a1_, o1_) u32x4 p0_, p1_; do { unsigned f_;                                                                 \
      do { asm volatile("ds_read_b32 %0, %3 offset:%8\n\tds_read_b128 %1, %4 offset:%6\n\tds_read_b128 %2, %5 offset:%7\n\ts_waitcnt lgkmcnt(0)" \
                        : "=&v"(f_), "=&v"(p0_), "=&v"(p1_) : "v"(fp), "v"(a0_), "v"(a1_), "n"(o0_), "n"(o1_), "n"(4 * (u_)) : "memory");        \
      } while (__builtin_amdgcn_readfirstlane(f_) == 0); } while (0)
#define UNIT1(u_, a0_, o0_) u32x4 p0_; do { unsigned f_;                                                                              \
      do { asm volatile("ds_read_b32 %0, %2 offset:%5\n\tds_read_b128 %1, %3 offset:%4\n\ts_waitcnt lgkmcnt(0)"                             \
                        : "=&v"(f_), "=&v"(p0_) : "v"(fp), "v"(a0_), "n"(o0_), "n"(4 * (u_)) : "memory");                                        \
      } while (__builtin_amdgcn_readfirstlane(f_) == 0); } while (0)
    { unsigned f_; do { asm volatile("ds_read_b32 %0, %1\n\ts_waitcnt lgkmcnt(0)" : "=v"(f_) : "v"(fp) : "memory"); } while (__builtin_amdgcn_readfirstlane(f_) == 0); }
    { UNIT(0, rw, 0, rw, 1024); O_ST(p0_, va[0], 0, AUX_NT); O_ST(p1_, va[1], 0, AUX_NT); }
    { UNIT(1, rw, 2048, rw, 3072); O_ST(p0_, va[2], 0, AUX_NT); O_ST(p1_, va[3], 0, AUX_NT); }
    { UNIT(2, rw, 4096, rw, 5120); O_ST(p0_, va[4], 0, AUX_NT); O_ST(p1_, va[5], 0, AUX_NT); }
    { UNIT(3, rw, 6144, rw, 7168); O_ST(p0_, va[6], 0, AUX_NT); O_ST(p1_, va[7], 0, AUX_NT); }
    { UNIT1(4, rs, 0); O_ST(p0_, va[8], 0, AUX_NT); }
    { UNIT1(5, rs, 2048); O_ST(p0_, va[9], 0, AUX_NT); }
    { UNIT1(6, rs, 4096); O_ST(p0_, va[10], 0, AUX_NT); }
    { UNIT1(7, rs, 6144); O_ST(p0_, va[11], 0, AUX_NT); }
  }
  asm volatile("" ::: "memory");
}

extern "C" void kernel_launch(void* const* d_in, const int* in_sizes, int n_in, void* d_out, int out_size, void* d_ws,
                              size_t ws_size, hipStream_t stream) {
  nf4_gemm<<<256, 512, 0, stream>>>((const uint16_t*)d_in[0], (const int*)d_in[1], (const uint16_t*)d_in[2],
                                    (const uint16_t*)d_in[3], (char*)d_ws, (uint16_t*)d_out);
}
```

```cpp
#include <hip/hip_runtime.h>
#include <hip/hip_bf16.h>
#include <stdint.h>

typedef __attribute__((ext_vector_type(8))) short bf16x8;
typedef __attribute__((ext_vector_type(4))) float f32x4;
typedef __attribute__((ext_vector_type(4))) int i32x4;
typedef __attribute__((ext_vector_type(4))) unsigned u32x4;
typedef __attribute__((ext_vector_type(2))) unsigned u32x2;
typedef __attribute__((ext_vector_type(2))) float f32x2;
typedef __attribute__((ext_vector_type(2))) __bf16 bf16x2_t;
typedef __attribute__((address_space(1))) unsigned gu32;

constexpr int M_ = 2048, K_ = 1024, N_ = 3584;
constexpr int BM = 256, BN = 112, BK = 64, NT = K_ / BK;
constexpr int XB = BM * BK * 2;
constexpr int WB = BN * BK * 2;
constexpr int KCB = BN * 16;
constexpr int SB = XB + WB;
constexpr int NS = 3;
constexpr int HDR = 4096;
constexpr int LUT_OFF = -HDR;
constexpr int FLG = 2560;
constexpr int CNT_OFF = -HDR + 2304;
constexpr int STG_OFF = NS * SB;
constexpr int SHM_BYTES = HDR + NS * SB + WB;
constexpr size_t WIMG_BYTES = (size_t)32 * NT * WB;
constexpr size_t SYNC_OFF = (size_t)8 << 20;
constexpr unsigned SPIN_MAX = 1u << 17;
constexpr unsigned CODE_TOUCH_MAX = 15364;
constexpr int AUX_NT = 2, AUX_SC1 = 16;

#define WAIT_V(n) asm volatile("s_waitcnt vmcnt(%0)" ::"n"(n) : "memory")
#define WAIT_L(n) asm volatile("s_waitcnt lgkmcnt(%0)" ::"n"(n) : "memory")
#define BAR() do { asm volatile("" ::: "memory"); __builtin_amdgcn_s_barrier(); asm volatile("" ::: "memory"); } while (0)
#define SCHED() __builtin_amdgcn_sched_barrier(0)
#define TAG_OF(e) ((e) * 0x9E3779B1u + 0x7F4A7C15u)
#define RLX_AGENT __ATOMIC_RELAXED, __HIP_MEMORY_SCOPE_AGENT

__device__ __forceinline__ unsigned pack_bf16(float a, float b) {
  f32x2 v = {a, b};
  bf16x2_t r = __builtin_convertvector(v, bf16x2_t);
  return __builtin_bit_cast(unsigned, r);
}
__device__ __forceinline__ unsigned long long order_of(int tm, int tn) {
  const int t = tn & 15;
  const unsigned long long a = ((0x765432765432ull >> (8 * (t % 3))) & 0xffffff) ^ ((t & 1) * 0x111111);
  const unsigned long long b = (0xFEDCBA9FEDCBA9ull >> (4 * (t % 7))) & 0xfffffff;
  return (0x810 | a << 12 | b << 36) ^ (tm * 0x1111111111111111ull);
}

struct QRegs { i32x4 r[8]; };
__device__ __forceinline__ float smul(float a, float b) { float r; asm("v_mul_f32 %0, %1, %2" : "=v"(r) : "v"(a), "v"(b)); return r; }

__global__ __launch_bounds__(512, 2) void nf4_gemm(const uint16_t* __restrict__ x, const int* __restrict__ qw,
                                                   const uint16_t* __restrict__ cb, const uint16_t* __restrict__ am,
                                                   char* __restrict__ ws, uint16_t* __restrict__ out) {
  __shared__ __attribute__((aligned(1024))) char shm_raw[SHM_BYTES];
  char* const shm = shm_raw + HDR;
  unsigned long long pc0; asm volatile("s_getpc_b64 %0" : "=s"(pc0));
  unsigned ct;
  asm volatile("global_load_dword %0, %1, %2" : "=&v"(ct) : "v"(min((unsigned)((threadIdx.x & 7) * 2048 + ((blockIdx.x >> 3) & 15) * 128), CODE_TOUCH_MAX)), "s"(pc0) : "memory");
  const int bid = blockIdx.x;
  const int xcd = bid & 7, bi = bid >> 3;
  const int tn = (xcd >> 2) * 16 + (bi & 15), tm = (xcd & 3) * 2 + ((bi >> 4) & 1);
  const int k1 = tm ^ 1, k2 = tm + 8;
  const int tid = threadIdx.x, lane = tid & 63;
  const int wid = __builtin_amdgcn_readfirstlane(tid >> 6);
  const bool qact = (lane & 31) < 28;
  const int nc = qact ? (lane & 31) : 27;
  const int kc0 = 2 * (wid & 3);
  const int tj = tn & 3, cb0 = (tn >> 2) * 448;
  const int cw = tj * 64, cs = 256 + tj * 32, cq = 384 + (tj ^ 2) * 16;
  const int col = nc * 4 + (nc < 16 ? cw : nc < 24 ? cs - 64 : cq - 96);
  const unsigned qvoff = (unsigned)(((kc0 + (lane >> 5)) * 8 * N_ + col) * 4);
  const unsigned avoff = (unsigned)(col * 2);
  const unsigned qs0 = (unsigned)(cb0 * 4);
  const int ci = (int)(0x7688854328888180ull >> (4 * (tn & 15)));
  const unsigned xtv = (unsigned)((((lane & 7) * (K_ * 2) + (((0x810 >> (4 * min(lane >> 3, 2))) & 15) ^ tm) * 128) & (ci & 8 ? 0 : -1)) + (tm * BM + (ci & 7) * 32 + (wid & 3) * 8) * (K_ * 2));
  asm volatile("" :: "v"(qvoff), "v"(avoff), "v"(xtv));
  asm volatile("" :: "s"(qs0 + (unsigned)tm * (BK * N_ * 4)));
  SCHED();
  asm volatile("" :: "s"(x), "s"(qw), "s"(cb), "s"(am), "s"(ws), "s"(out));
  gu32* flags = (gu32*)(ws + SYNC_OFF + (size_t)tn * 2048);
  typedef __attribute__((address_space(3))) unsigned lu32;
  lu32* cnt = (lu32*)(shm + CNT_OFF);
#define SIGNAL(v_) asm volatile("s_mov_b64 exec, 1\n\tds_add_u32 %0, %1\n\ts_mov_b64 exec, -1" :: "v"(cnt), "v"(v_) : "memory")
#define AWAIT(m_, n_) do { while ((__hip_atomic_load(cnt, __ATOMIC_RELAXED, __HIP_MEMORY_SCOPE_WORKGROUP) & (m_)) < (n_)) {}     \
      asm volatile("" ::: "memory"); SCHED(); } while (0)
  char* lut = shm + LUT_OFF;
  const __amdgpu_buffer_rsrc_t rsW = __builtin_amdgcn_make_buffer_rsrc((void*)ws, (short)0, (int)WIMG_BYTES, 0x00020000);
  const __amdgpu_buffer_rsrc_t rsQ = __builtin_amdgcn_make_buffer_rsrc((void*)qw, (short)0, K_ * N_ * 4, 0x00020000);
  const uint16_t* am0 = am + cb0;
  const unsigned zero = 0;
#define Q_ROW(Q, j, so_) asm volatile("buffer_load_dwordx4 %0, %1, %2, %3 offen" : "=&v"(Q.r[j]) : "v"(qvoff), "s"(rsQ), "s"((so_) + (j) * (N_ * 4)) : "memory")
#define Q_ROWS(Q, so_) do { _Pragma("unroll") for (int j_ = 0; j_ < 8; j_++) Q_ROW(Q, j_, so_); } while (0)
#define Q_SC(S, ks) asm volatile("global_load_dwordx2 %0, %1, %2" : "=&v"(S) : "v"(avoff), "s"(am0 + (ks) * N_) : "memory")
#define Q_SO(ks) (qs0 + (unsigned)(ks) * (BK * N_ * 4))
#define TAG_LOAD(T) asm volatile("global_load_dword %0, %1, %2 sc1" : "=&v"(T) : "v"(zero), "s"(flags) : "memory")
#define SCALES(S) do { asm volatile("" : "+v"(S));                                                                                \
      sc[0] = __builtin_bit_cast(float, S.x << 16); sc[1] = __builtin_bit_cast(float, S.x & 0xffff0000u);                         \
      sc[2] = __builtin_bit_cast(float, S.y << 16); sc[3] = __builtin_bit_cast(float, S.y & 0xffff0000u); } while (0)
#define R_LOOK(Q, a_, n_) do { WAIT_V(n_); asm volatile("" : "+v"(Q.r[2 * (a_)]), "+v"(Q.r[2 * (a_) + 1]));                        \
      _Pragma("unroll") for (int i_ = 0; i_ < 4; i_ += 2) { u32x2 d_;                                                             \
        asm("v_lshl_add_u64 %0, %1, 4, %2\n\tv_lshl_add_u64 %0, %0, 3, 0" : "=&v"(d_)                                            \
            : "v"((u32x2){(unsigned)Q.r[2 * (a_) + 1][i_], (unsigned)Q.r[2 * (a_) + 1][i_ + 1]}),                                 \
              "v"((u32x2){(unsigned)Q.r[2 * (a_)][i_], (unsigned)Q.r[2 * (a_)][i_ + 1]}));                                        \
        lv[i_][a_] = *(const f32x2*)(lut + d_.x); lv[i_ + 1][a_] = *(const f32x2*)(lut + d_.y); }                                 \
      SCHED(); } while (0)
#define R_PACK(a_, sm) do { _Pragma("unroll") for (int i_ = 0; i_ < 4; i_++)                                                      \
      po[i_][a_] = (sm) ? pack_bf16(smul(lv[i_][a_].x, sc[i_]), smul(lv[i_][a_].y, sc[i_])) : pack_bf16(lv[i_][a_].x * sc[i_], lv[i_][a_].y * sc[i_]); \
      SCHED(); } while (0)
#define R_WRITE(dst) do { _Pragma("unroll") for (int i_ = 0; i_ < 4; i_++) *(u32x4*)(shm + (dst) + (i_ < 2 ? io0 : io1) + i_ * 896) = po[i_]; SCHED(); } while (0)
#define ROUND(Q, S, dst, e, sm) do { R_LOOK(Q, 0, (e) + 6); SCALES(S); R_LOOK(Q, 1, (e) + 4); R_PACK(0, sm);                      \
      R_LOOK(Q, 2, (e) + 2); R_PACK(1, sm); R_LOOK(Q, 3, e); R_PACK(2, sm); R_PACK(3, sm); R_WRITE(dst); } while (0)
#define PUBLISH(src, ks, kcp) do { WAIT_L(0); if (lane < 56) {                                                                    \
        const unsigned g_ = (unsigned)((tn * NT + (ks)) * WB + (kcp) * (2 * KCB) + lane * 16); u32x4 pv_[4];                      \
        _Pragma("unroll") for (int i_ = 0; i_ < 4; i_++) pv_[i_] = *(const u32x4*)(shm + (src) + lane * 16 + i_ * 896);           \
        _Pragma("unroll") for (int i_ = 0; i_ < 4; i_++) __builtin_amdgcn_raw_buffer_store_b128(pv_[i_], rsW, g_ + i_ * 896, 0, AUX_SC1); } \
      SCHED(); } while (0)
  f32x2 lv[4][4]; float sc[4]; u32x4 po[4];
  const __amdgpu_buffer_rsrc_t rsO = __builtin_amdgcn_make_buffer_rsrc((void*)out, (short)0, M_ * N_ * 2, 0x00020000);
#define O_ADDR(vo, ro, k) unsigned vo; int ro; do { const int r_ = lane >> (3 - (k)), c_ = lane & (7 >> (k));                     \
      vo = (unsigned)(((tm * BM + (wid & 3) * 64 + r_) * N_ + cb0 + ((k) == 0 ? cw : (k) == 1 ? cs : cq)) * 2 + c_ * 16);     \
      ro = HDR + ((k) ? 0 : SB) + (wid & 3) * 8192 + r_ * 128 + (((c_ + ((k) & 2) * 2) ^ (r_ & 7)) << 4);                         \
      asm volatile("" : "+v"(vo), "+v"(ro)); } while (0)
#define O_RD(ro, r_) (*(const u32x4*)(shm_raw + (ro) + (r_) * 128))
#define O_ST(v_, vo, r_, aux) __builtin_amdgcn_raw_buffer_store_b128(v_, rsO, (vo) + (r_) * (N_ * 2), 0, aux)
#define IO_OF() const int io0 = nc * 16 + (lane >> 5) * 448, io1 = nc * 16 + 448 - (lane >> 5) * 448

  if (__builtin_expect(wid < 4, 1)) {
    QRegs q; u32x2 qsv; unsigned c0, c1, tagv, xt0;
    asm volatile("global_load_ushort %0, %2, %4\n\tglobal_load_ushort %1, %3, %4" : "=&v"(c0), "=&v"(c1)
                 : "v"((unsigned)((lane & 15) * 2)), "v"((unsigned)((wid * 4 + (lane >> 4)) * 2)), "s"(cb) : "memory");
    TAG_LOAD(tagv);
    Q_SC(qsv, tm);
    asm volatile("global_load_dword %0, %1, %2" : "=&v"(xt0) : "v"(xtv), "s"(x) : "memory");
    Q_ROWS(q, Q_SO(tm));
    SCHED();
    IO_OF();
    const unsigned gb = (unsigned)((tn * NT + tm) * WB + kc0 * KCB);
    unsigned go0 = qact ? gb + io0 : 0x7fffffffu, go1 = qact ? gb + io1 : 0x7fffffffu;
    asm volatile("" : "+v"(go0), "+v"(go1));
    f32x4 acc[4][7];
#pragma unroll
    for (int m = 0; m < 4; m++)
#pragma unroll
      for (int n = 0; n < 7; n++) { acc[m][n] = (f32x4){0.f, 0.f, 0.f, 0.f}; asm volatile("" : "+v"(acc[m][n])); }
    WAIT_V(11);
    asm volatile("" : "+v"(c0), "+v"(c1)); asm volatile("" :: "v"(ct));
    *cnt = 0;
    *(u32x2*)(lut + tid * 8) = (u32x2){c0 << 16, c1 << 16};
    int fa = FLG + wid * 320 + lane * 4;
    asm volatile("" : "+v"(fa));
    *(unsigned*)(shm_raw + fa + 64) = 0u;
    WAIT_L(0);
    BAR();
    ROUND(q, qsv, XB + kc0 * KCB, 0, 0);
    WAIT_L(0);
    SIGNAL(1u);
    SCHED();
    __builtin_amdgcn_raw_buffer_store_b128(po[0], rsW, go0, 0, AUX_SC1);
    __builtin_amdgcn_raw_buffer_store_b128(po[1], rsW, go0 + 896, 0, AUX_SC1);
    __builtin_amdgcn_raw_buffer_store_b128(po[2], rsW, go1 + 2 * 896, 0, AUX_SC1);
    __builtin_amdgcn_raw_buffer_store_b128(po[3], rsW, go1 + 3 * 896, 0, AUX_SC1);
    SCHED();
    const int fr = lane & 15, fq = lane >> 4;
    const int xo0 = (wid * 64 + fr) * 128 + ((fq ^ (fr >> 1)) << 4), xo1 = xo0 ^ 64;
    const int wo = XB + (fq >> 1) * (2 * KCB) + (fr & 3) * 896 + ((fq ^ (fr >> 1)) & 1) * 448 + (fr >> 2) * 16;
    bf16x8 xa[4], wa[7], xb[4], wb[7];
#define LD(o_) (*(const bf16x8*)(shm + (o_)))
#define MF(m_, n_, xv, w_) do { asm volatile("v_mfma_f32_16x16x32_bf16 %0, %1, %2, %0" : "+v"(acc[m_][n_]) : "v"(w_[n_]), "v"(xv)); SCHED(); } while (0)
#define MF2(m_, n_, x1, w1, x2, w2) do { asm volatile("v_mfma_f32_16x16x32_bf16 %0, %1, %2, %0\n\tv_mfma_f32_16x16x32_bf16 %0, %3, %4, %0" \
      : "+v"(acc[m_][n_]) : "v"(w1[n_]), "v"(x1), "v"(w2[n_]), "v"(x2)); SCHED(); } while (0)
#define MF3(m_, n_, x1, w1, x2, w2, x3, w3) do { asm volatile("v_mfma_f32_16x16x32_bf16 %0, %1, %2, %0\n\tv_mfma_f32_16x16x32_bf16 %0, %3, %4, %0\n\tv_mfma_f32_16x16x32_bf16 %0, %5, %6, %0" \
      : "+v"(acc[m_][n_]) : "v"(w1[n_]), "v"(x1), "v"(w2[n_]), "v"(x2), "v"(w3[n_]), "v"(x3)); SCHED(); } while (0)
#define RX(x_, b_, m_) x_[m_] = LD((b_) + (m_) * 2048)
#define RW(w_, b_, hf, n_) w_[n_] = LD((b_) + (hf) * (4 * KCB) + (n_) * 64)
#define WL(n_) do { SCHED(); __builtin_amdgcn_s_waitcnt(0xC07F | ((n_) << 8)); SCHED(); } while (0)
    O_ADDR(vq, rq, 2);
    int e0 = HDR + wid * 8192 + fr * 128 + (((fq >> 1) ^ (fr & 7)) << 4) + (fq & 1) * 8, e1 = e0 ^ 32, e2 = e0 ^ 64, e3 = e0 ^ 96;
    asm volatile("" : "+v"(e0), "+v"(e1), "+v"(e2), "+v"(e3));
    AWAIT(15u, 6u);
#define STAGE(st) do { int aw_ = wo + (st) * SB; asm volatile("" : "+v"(aw_)); \
      RX(xa, xo0 + (st) * SB, 0); SCHED(); RW(wa, aw_, 0, 0); SCHED(); RX(xb, xo1 + (st) * SB, 0); SCHED(); RW(wb, aw_, 1, 0); SCHED(); \
      RW(wb, aw_, 1, 1); SCHED(); RW(wa, aw_, 0, 1); SCHED(); RW(wa, aw_, 0, 2); SCHED(); RW(wb, aw_, 1, 2); SCHED(); \
      RW(wb, aw_, 1, 3); SCHED(); RW(wa, aw_, 0, 3); SCHED(); \
      MF(0, 0, xa[0], wa); RX(xa, xo0 + (st) * SB, 1); SCHED(); \
      MF(0, 0, xb[0], wb); RX(xb, xo1 + (st) * SB, 1); SCHED(); \
      MF(0, 1, xb[0], wb); RX(xa, xo0 + (st) * SB, 2); SCHED(); \
      MF(0, 1, xa[0], wa); RX(xb, xo1 + (st) * SB, 2); SCHED(); \
      MF(0, 2, xa[0], wa); RX(xa, xo0 + (st) * SB, 3); SCHED(); \
      MF(0, 2, xb[0], wb); RX(xb, xo1 + (st) * SB, 3); SCHED(); \
      MF(0, 3, xb[0], wb); RW(wa, aw_, 0, 4); SCHED(); \
      MF(0, 3, xa[0], wa); RW(wb, aw_, 1, 4); SCHED(); \
      MF(1, 3, xa[1], wa); RW(wb, aw_, 1, 5); SCHED(); \
      MF(1, 3, xb[1], wb); RW(wa, aw_, 0, 5); SCHED(); \
      MF(1, 2, xb[1], wb); RW(wa, aw_, 0, 6); SCHED(); \
      MF(1, 2, xa[1], wa); RW(wb, aw_, 1, 6); SCHED(); \
      MF2(1, 1, xa[1], wa, xb[1], wb); \
      MF2(1, 0, xb[1], wb, xa[1], wa); \
      MF2(2, 0, xa[2], wa, xb[2], wb); \
      MF2(2, 1, xb[2], wb, xa[2], wa); \
      MF2(2, 2, xa[2], wa, xb[2], wb); \
      MF2(2, 3, xb[2], wb, xa[2], wa); \
      MF2(3, 3, xa[3], wa, xb[3], wb); \
      MF2(3, 2, xb[3], wb, xa[3], wa); \
      if ((st) == 0) {                            \
        WAIT_V(0); asm volatile("" : "+v"(tagv) : "v"(xt0)); \
        if (lane == 0) __hip_atomic_store(flags + 256 + tm * 32 + wid, TAG_OF(tagv), RLX_AGENT); } \
      MF2(3, 1, xa[3], wa, xb[3], wb); \
      MF2(3, 0, xb[3], wb, xa[3], wa); \
      MF2(0, 4, xa[0], wa, xb[0], wb); \
      MF2(0, 5, xb[0], wb, xa[0], wa); \
      MF2(0, 6, xa[0], wa, xb[0], wb); \
      MF2(1, 6, xb[1], wb, xa[1], wa); \
      MF2(1, 5, xa[1], wa, xb[1], wb); \
      MF2(1, 4, xb[1], wb, xa[1], wa); \
      MF2(2, 4, xa[2], wa, xb[2], wb); \
      MF2(2, 5, xb[2], wb, xa[2], wa); \
      MF2(2, 6, xa[2], wa, xb[2], wb); \
      MF2(3, 6, xb[3], wb, xa[3], wa); \
      MF2(3, 5, xa[3], wa, xb[3], wb); \
      MF2(3, 4, xb[3], wb, xa[3], wa); \
      } while (0)
    STAGE(0);
    BAR();
    STAGE(1);
    asm volatile(".rept 4\n\ts_nop 0\n\t.endr");
    BAR();
    int aw = wo + STG_OFF - XB, a0 = xo0 + 2 * SB, a1 = xo1 + 2 * SB, so = 2 * SB;
    asm volatile("" : "+v"(aw), "+v"(a0), "+v"(a1));
      RW(wa, aw, 0, 1); SCHED(); RW(wb, aw, 1, 0); SCHED(); RW(wa, aw, 0, 0); SCHED(); RW(wb, aw, 1, 1); SCHED();
      RX(xa, a0, 0); SCHED(); RX(xb, a1, 0); SCHED(); RW(wa, aw, 0, 2); SCHED(); RW(wb, aw, 1, 2); SCHED();
      RW(wb, aw, 1, 3); SCHED(); RW(wa, aw, 0, 3); SCHED(); RX(xa, a0, 1); SCHED(); RX(xb, a1, 1); SCHED();
      RX(xa, a0, 2); SCHED(); RX(xb, a1, 2); SCHED();
    WL(5);
#pragma unroll 1
    for (int t = NT - 5; t;) {
      MF(0, 0, xa[0], wa); RX(xa, a0, 3); SCHED();
      MF(0, 0, xb[0], wb);
      MF(0, 1, xb[0], wb); RX(xb, a1, 3); SCHED();
      MF(0, 1, xa[0], wa);
      MF(0, 2, xa[0], wa); RW(wa, aw, 0, 4); SCHED();
      MF(0, 2, xb[0], wb); WL(4); SCHED();
      MF(0, 3, xb[0], wb); RW(wb, aw, 1, 4); SCHED();
      MF(0, 3, xa[0], wa);
      MF(1, 3, xa[1], wa); RW(wb, aw, 1, 5); SCHED();
      MF(1, 3, xb[1], wb);
      MF(1, 2, xb[1], wb); RW(wa, aw, 0, 5); SCHED();
      MF(1, 2, xa[1], wa);
      MF(1, 1, xa[1], wa); RW(wa, aw, 0, 6); SCHED();
      MF(1, 1, xb[1], wb);
      MF(1, 0, xb[1], wb); RW(wb, aw, 1, 6); SCHED();
      MF(1, 0, xa[1], wa); so = so == 2 * SB ? 0 : so + SB; asm volatile("" : "+s"(so)); SCHED();
      MF(2, 0, xa[2], wa); WL(3); aw = wo + so; a0 = xo0 + so; asm volatile("" : "+v"(aw), "+v"(a0)); SCHED();
      MF(2, 0, xb[2], wb); a1 = xo1 + so; asm volatile("" : "+v"(a1)); SCHED();
      MF2(2, 1, xb[2], wb, xa[2], wa);
      MF2(2, 2, xa[2], wa, xb[2], wb);
      MF2(2, 3, xb[2], wb, xa[2], wa);
      MF2(3, 3, xa[3], wa, xb[3], wb);
      MF(3, 2, xb[3], wb); WL(0); SCHED();
      MF(3, 2, xa[3], wa); BAR(); SCHED();
      MF(3, 1, xa[3], wa); RW(wa, aw, 0, 1); SCHED();
      MF(3, 1, xb[3], wb);
      MF(3, 0, xb[3], wb); RW(wb, aw, 1, 0); SCHED();
      MF(3, 0, xa[3], wa);
      MF(0, 4, xa[0], wa); RW(wa, aw, 0, 0); SCHED();
      MF(0, 4, xb[0], wb);
      MF(0, 5, xb[0], wb); RW(wb, aw, 1, 1); SCHED();
      MF(0, 5, xa[0], wa);
      MF(0, 6, xa[0], wa); RX(xa, a0, 0); SCHED();
      MF(0, 6, xb[0], wb);
      MF(1, 6, xb[1], wb); RX(xb, a1, 0); SCHED();
      MF(1, 6, xa[1], wa);
      MF(1, 5, xa[1], wa); RW(wa, aw, 0, 2); SCHED();
      MF(1, 5, xb[1], wb);
      MF(1, 4, xb[1], wb); RW(wb, aw, 1, 2); SCHED();
      MF(1, 4, xa[1], wa);
      MF(2, 4, xa[2], wa); RW(wb, aw, 1, 3); SCHED();
      MF(2, 4, xb[2], wb);
      MF(2, 5, xb[2], wb); RW(wa, aw, 0, 3); SCHED();
      MF(2, 5, xa[2], wa);
      MF(2, 6, xa[2], wa); RX(xa, a0, 1); SCHED();
      MF(2, 6, xb[2], wb);
      MF(3, 6, xb[3], wb); RX(xb, a1, 1); SCHED();
      MF(3, 6, xa[3], wa); t--; asm volatile("" : "+s"(t)); SCHED();
      MF(3, 5, xa[3], wa); RX(xa, a0, 2); SCHED();
      MF(3, 5, xb[3], wb); WL(4); SCHED();
      MF(3, 4, xb[3], wb); RX(xb, a1, 2); SCHED();
      MF(3, 4, xa[3], wa);
    }
      MF(0, 0, xa[0], wa); RX(xa, a0, 3); SCHED();
      MF(0, 0, xb[0], wb);
      MF(0, 1, xb[0], wb); RX(xb, a1, 3); SCHED();
      MF(0, 1, xa[0], wa);
      MF(0, 2, xa[0], wa); int a5 = aw + SB + WB; asm volatile("" : "+v"(a5)); SCHED();
      MF(0, 2, xb[0], wb);
      MF(0, 3, xb[0], wb); WL(2); SCHED();
      MF(0, 3, xa[0], wa);
      MF2(1, 3, xa[1], wa, xb[1], wb);
      BAR();
    SCHED();
#define CV(m_, j_) pk[m_][j_] = pack_bf16(acc[m_][(j_) / 2][((j_) & 1) * 2], acc[m_][(j_) / 2][((j_) & 1) * 2 + 1])
#define W64(m_, n_) *(u32x2*)(shm_raw + (((n_) & 3) == 0 ? e0 : ((n_) & 3) == 1 ? e1 : ((n_) & 3) == 2 ? e2 : e3) + ((n_) < 4 ? SB : 0) + (m_) * 2048) = (u32x2){pk[m_][2 * (n_)], pk[m_][2 * (n_) + 1]}
#define W32(m_, n_, h_) *(unsigned*)(shm_raw + (((n_) & 3) == 0 ? e0 : ((n_) & 3) == 1 ? e1 : ((n_) & 3) == 2 ? e2 : e3) + ((n_) < 4 ? SB : 0) + (m_) * 2048 + 4 * (h_)) = pk[m_][2 * (n_) + (h_)]
#define WA(m_, n_) W32(m_, n_, 0)
#define WB_(m_, n_) W32(m_, n_, 1)
#define FLAG(u_) do { asm volatile("" ::: "memory"); *(int*)(shm_raw + fa + 4 * (u_)) = fa; asm volatile("" ::: "memory"); } while (0)
    {
      bf16x8 xc[4], xd[4], xe[4], xf[4], wc[7], wd[7], we[7], wf[7], wg[7], wh[7];
      unsigned pk[4][14];
      u32x4 ov[2];
      MF(1, 2, xb[1], wb); RX(xc, a0 + SB, 0); SCHED();
      MF(1, 2, xa[1], wa); RW(wc, aw + SB, 0, 0); SCHED();
      MF(1, 1, xa[1], wa); RX(xd, a1 + SB, 0); SCHED();
      MF(1, 1, xb[1], wb); RW(wd, aw + SB, 1, 0); SCHED();
      MF(1, 0, xb[1], wb); RW(wd, aw + SB, 1, 1); SCHED();
      MF(1, 0, xa[1], wa);
      MF(2, 0, xa[2], wa); RW(wc, aw + SB, 0, 1); SCHED();
      MF(2, 0, xb[2], wb); RW(wc, aw + SB, 0, 2); SCHED();
      MF(2, 1, xb[2], wb); RW(wd, aw + SB, 1, 2); SCHED();
      MF(2, 1, xa[2], wa); RW(wd, aw + SB, 1, 3); SCHED();
      MF2(2, 2, xa[2], wa, xb[2], wb); RW(wc, aw + SB, 0, 3); SCHED();
      MF(2, 3, xb[2], wb); RW(we, a5, 0, 0); SCHED();
      MF(2, 3, xa[2], wa); RW(wf, a5, 1, 0); WL(5); SCHED();
      MF(3, 3, xa[3], wa); RW(wf, a5, 1, 1); SCHED();
      MF(3, 3, xb[3], wb);
      MF(3, 2, xb[3], wb); RW(we, a5, 0, 1); SCHED();
      MF(3, 2, xa[3], wa); RW(we, a5, 0, 2); SCHED();
      MF(3, 1, xa[3], wa); RW(wf, a5, 1, 2); SCHED();
      MF(3, 1, xb[3], wb); RW(wf, a5, 1, 3); SCHED();
      MF2(3, 0, xb[3], wb, xa[3], wa); RW(we, a5, 0, 3); SCHED();
      BAR();
      SCHED();
      MF(0, 0, xc[0], wc); RX(xe, xo0, 0); RX(xf, xo1, 0); SCHED();
      MF(0, 0, xd[0], wd);
      MF(0, 1, xd[0], wd); RX(xc, a0 + SB, 1); SCHED();
      MF(0, 1, xc[0], wc); RX(xd, a1 + SB, 1); SCHED();
      MF(0, 2, xc[0], wc); RX(xe, xo0, 1); WL(6); SCHED();
      MF(0, 2, xd[0], wd); RX(xf, xo1, 1); SCHED();
      MF2(0, 3, xd[0], wd, xc[0], wc); WL(4); SCHED();
      MF2(0, 0, xe[0], we, xf[0], wf);
      MF2(0, 1, xf[0], wf, xe[0], we); WL(0); SCHED();
      MF(0, 2, xe[0], we); CV(0, 0); CV(0, 1); SCHED();
      MF(0, 2, xf[0], wf); W64(0, 0); SCHED();
      MF(0, 3, xf[0], wf); CV(0, 2); CV(0, 3); SCHED();
      MF(0, 3, xe[0], we); W64(0, 1); SCHED();
      MF(1, 0, xc[1], wc); CV(0, 4); CV(0, 5); SCHED();
      MF(1, 0, xd[1], wd); W64(0, 2); SCHED();
      MF(1, 0, xe[1], we); CV(0, 6); CV(0, 7); SCHED();
      MF(1, 0, xf[1], wf); W64(0, 3); SCHED();
      MF(1, 1, xf[1], wf); RX(xc, a0 + SB, 2); RX(xd, a1 + SB, 2); SCHED();
      MF2(1, 1, xe[1], we, xd[1], wd); CV(1, 0); CV(1, 1); SCHED();
      MF(1, 1, xc[1], wc); W64(1, 0); SCHED();
      MF(1, 2, xc[1], wc); WL(3); SCHED();
      MF(1, 2, xd[1], wd); BAR(); SCHED();
      MF(1, 2, xe[1], we); CV(1, 2); CV(1, 3); SCHED();
      MF(1, 2, xf[1], wf); W64(1, 1); SCHED();
      MF(1, 3, xf[1], wf); RX(xe, xo0, 2); RX(xf, xo1, 2); SCHED();
      MF(1, 3, xe[1], we); WL(4); SCHED();
      MF(1, 3, xd[1], wd); CV(1, 4); CV(1, 5); SCHED();
      MF(1, 3, xc[1], wc); W64(1, 2); SCHED();
      MF(2, 0, xc[2], wc); RX(xc, a0 + SB, 3); SCHED();
      MF(2, 0, xd[2], wd); RX(xd, a1 + SB, 3); WL(3); SCHED();
      MF(2, 0, xe[2], we); CV(1, 6); CV(1, 7); SCHED();
      MF(2, 0, xf[2], wf); W64(1, 3); SCHED();
      MF(2, 1, xf[2], wf); RX(xe, xo0, 3); SCHED();
      MF(2, 1, xe[2], we); RX(xf, xo1, 3); SCHED();
      MF(2, 1, xd[2], wd); CV(2, 0); CV(2, 1); SCHED();
      MF(2, 1, xc[2], wc); W64(2, 0); SCHED();
      MF(2, 2, xc[2], wc); WL(3); SCHED();
      MF(2, 2, xd[2], wd); BAR(); SCHED();
      MF(2, 2, xe[2], we); CV(2, 2); CV(2, 3); SCHED();
      MF(2, 2, xf[2], wf); W64(2, 1); SCHED();
      MF(2, 3, xf[2], wf); RW(wg, aw, 0, 4); SCHED();
      MF(2, 3, xe[2], we); WL(3); SCHED();
      MF(2, 3, xd[2], wd); CV(2, 4); CV(2, 5); SCHED();
      MF(2, 3, xc[2], wc); W64(2, 2); SCHED();
      MF(3, 0, xc[3], wc); RW(wh, aw, 1, 4); SCHED();
      MF(3, 0, xd[3], wd); RW(wc, aw + SB, 0, 4); SCHED();
      MF(3, 0, xe[3], we); CV(2, 6); CV(2, 7); SCHED();
      MF(3, 0, xf[3], wf); W64(2, 3); SCHED();
      MF(3, 1, xf[3], wf); RW(wd, aw + SB, 1, 4); SCHED();
      MF(3, 1, xe[3], we); RW(we, a5, 0, 4); SCHED();
      MF(3, 1, xd[3], wd); CV(3, 0); CV(3, 1); SCHED();
      MF(3, 1, xc[3], wc); W64(3, 0); SCHED();
      MF(3, 2, xc[3], wc); RW(wf, a5, 1, 4); WL(4); SCHED();
      MF(3, 2, xd[3], wd); BAR(); RW(wf, a5, 1, 5); SCHED();
      MF(3, 2, xe[3], we); CV(3, 2); CV(3, 3); SCHED();
      MF(3, 2, xf[3], wf); W64(3, 1); SCHED();
      MF(3, 3, xf[3], wf); RW(we, a5, 0, 5); RW(wd, aw + SB, 1, 5); SCHED();
      MF(3, 3, xe[3], we); RW(wc, aw + SB, 0, 5); WL(7); SCHED();
      MF(3, 3, xd[3], wd); CV(3, 4); CV(3, 5); SCHED();
      MF(3, 3, xc[3], wc); W64(3, 2); SCHED();
      MF(0, 4, xa[0], wg); RW(wg, aw, 0, 5); RW(wh, aw, 1, 5); SCHED();
      MF(0, 4, xb[0], wh); WL(7); SCHED();
      MF(0, 4, xc[0], wc); CV(3, 6); CV(3, 7); SCHED();
      MF(0, 4, xd[0], wd); W64(3, 3); SCHED();
      MF(0, 4, xe[0], we); FLAG(4); SCHED();
      MF(0, 4, xf[0], wf);
      MF(0, 5, xf[0], wf); WL(2); SCHED();
      MF2(0, 5, xe[0], we, xd[0], wd); RW(wg, aw, 0, 6); CV(0, 8); SCHED();
      MF(0, 5, xc[0], wc); WA(0, 4); SCHED();
      MF(0, 5, xa[0], wg); RW(wh, aw, 1, 6); CV(0, 9); SCHED();
      MF(0, 5, xb[0], wh); WB_(0, 4); SCHED();
      MF(1, 4, xb[1], wh); RW(wc, aw + SB, 0, 6); SCHED();
      MF(1, 4, xa[1], wg); RW(wd, aw + SB, 1, 6); SCHED();
      MF(1, 4, xc[1], wc); RW(we, a5, 0, 6); CV(0, 10); SCHED();
      MF(1, 4, xd[1], wd); WA(0, 5); SCHED();
      MF(1, 4, xe[1], we); RW(wf, a5, 1, 6); CV(0, 11); SCHED();
      MF(1, 4, xf[1], wf); WB_(0, 5); SCHED();
      MF(1, 5, xf[1], wf); FLAG(5); SCHED();
      MF2(1, 5, xe[1], we, xd[1], wd); CV(1, 8); SCHED();
      MF(1, 5, xc[1], wc); WA(1, 4); SCHED();
      MF(1, 5, xb[1], wh); CV(1, 9); SCHED();
      MF(1, 5, xa[1], wg); WB_(1, 4); SCHED();
      MF3(2, 4, xa[2], wg, xb[2], wh, xc[2], wc); CV(1, 10); SCHED();
      MF(2, 4, xd[2], wd); WA(1, 5); SCHED();
      MF(2, 4, xe[2], we); CV(1, 11); SCHED();
      MF(2, 4, xf[2], wf); WB_(1, 5); SCHED();
      MF(2, 5, xf[2], wf); FLAG(6); SCHED();
      MF2(2, 5, xe[2], we, xd[2], wd); CV(2, 8); SCHED();
      MF(2, 5, xc[2], wc); WA(2, 4); SCHED();
      MF(2, 5, xa[2], wg); CV(2, 9); SCHED();
      MF(2, 5, xb[2], wh); WB_(2, 4); SCHED();
      MF3(3, 4, xb[3], wh, xa[3], wg, xc[3], wc); CV(2, 10); SCHED();
      MF(3, 4, xd[3], wd); WA(2, 5); SCHED();
      MF(3, 4, xe[3], we); CV(2, 11); SCHED();
      MF(3, 4, xf[3], wf); WB_(2, 5); SCHED();
      MF(3, 5, xf[3], wf); FLAG(7); SCHED();
      MF2(3, 5, xe[3], we, xd[3], wd); CV(3, 8); SCHED();
      MF(3, 5, xc[3], wc); WA(3, 4); SCHED();
      MF(3, 5, xb[3], wh); WL(13); CV(3, 9); SCHED();
      MF(3, 5, xa[3], wg); WB_(3, 4); SCHED();
      MF3(0, 6, xa[0], wg, xb[0], wh, xc[0], wc); CV(3, 10); SCHED();
      MF(0, 6, xd[0], wd); WA(3, 5); SCHED();
      MF(0, 6, xe[0], we); CV(3, 11); SCHED();
      MF(0, 6, xf[0], wf); WB_(3, 5); SCHED();
      MF(1, 6, xf[1], wf); FLAG(8); SCHED();
      MF2(1, 6, xe[1], we, xd[1], wd); CV(0, 12); SCHED();
      MF(1, 6, xc[1], wc); WA(0, 6); SCHED();
      MF(1, 6, xa[1], wg); CV(0, 13); SCHED();
      MF(1, 6, xb[1], wh); WB_(0, 6); SCHED();
      MF3(2, 6, xa[2], wg, xb[2], wh, xc[2], wc); CV(1, 12); SCHED();
      MF(2, 6, xd[2], wd); WA(1, 6); SCHED();
      MF(2, 6, xe[2], we); CV(1, 13); SCHED();
      MF(2, 6, xf[2], wf); WB_(1, 6); SCHED();
      MF(3, 6, xf[3], wf); ov[0] = O_RD(rq, 0); SCHED();
      MF2(3, 6, xe[3], we, xd[3], wd); CV(2, 12); CV(2, 13); SCHED();
      MF(3, 6, xc[3], wc); WA(2, 6); SCHED();
      MF(3, 6, xa[3], wg); WB_(2, 6); SCHED();
      MF(3, 6, xb[3], wh);
      O_ST(ov[0], vq, 0, 0); SCHED();
      asm volatile("s_nop 6");
      SCHED();
      CV(3, 12); CV(3, 13); W64(3, 6); ov[1] = O_RD(rq, 32); SCHED();
      O_ST(ov[1], vq, 32, 0);
    }
  } else {
    IO_OF();
    const unsigned long long ord = order_of(tm, tn);
#define KS(s_) ((int)(ord >> (4 * (s_))) & 15)
    const int h = wid & 1;
    unsigned xv[16];
#pragma unroll
    for (int i = 0; i < 16; i++) xv[i] = (unsigned)((8 * i + (lane >> 3)) * (K_ * 2) + (((lane & 7) ^ ((i & 1) * 4 + (lane >> 4))) << 4));
    const char* xbase = (const char*)(x + (size_t)(tm * BM + h * 128) * K_);
    const unsigned ldsx = (unsigned)(size_t)(shm) + h * 16384;
#define XJ(o, j, l_, b_)                                                                                                          \
      asm volatile("s_mov_b32 m0, %8\n\ts_nop 0\n\tglobal_load_lds_dwordx4 %0, %9\n\t"                                            \
                   "s_add_u32 m0, m0, 0x400\n\ts_nop 0\n\tglobal_load_lds_dwordx4 %1, %9\n\t"                                     \
                   "s_add_u32 m0, m0, 0x400\n\ts_nop 0\n\tglobal_load_lds_dwordx4 %2, %9\n\t"                                     \
                   "s_add_u32 m0, m0, 0x400\n\ts_nop 0\n\tglobal_load_lds_dwordx4 %3, %9\n\t"                                     \
                   "s_add_u32 m0, m0, %10\n\ts_nop 0\n\tglobal_load_lds_dwordx4 %4, %9\n\t"                                       \
                   "s_add_u32 m0, m0, 0x400\n\ts_nop 0\n\tglobal_load_lds_dwordx4 %5, %9\n\t"                                     \
                   "s_add_u32 m0, m0, 0x400\n\ts_nop 0\n\tglobal_load_lds_dwordx4 %6, %9\n\t"                                     \
                   "s_add_u32 m0, m0, 0x400\n\ts_nop 0\n\tglobal_load_lds_dwordx4 %7, %9"                                         \
                   :: "v"(xv[o]), "v"(xv[o + 1]), "v"(xv[o + 2]), "v"(xv[o + 3]), "v"(xv[o + j]), "v"(xv[o + j + 1]), "v"(xv[o + j + 2]), "v"(xv[o + j + 3]), \
                      "s"(l_), "s"(b_), "n"((j - 3) * 1024) : "memory", "scc")
#define X8(o, l_, b_) XJ(o, 4, l_, b_)
#define ISSUE_X(s, slot) do { const char* b_ = xbase + KS(s) * 128; const unsigned l_ = ldsx + (slot) * SB;                       \
      X8(0, l_, b_); X8(8, l_ + 8192, b_); } while (0)
    const int fj = (int)(ord >> (4 * min(lane >> 2, 12) + 12));
    const unsigned fvo = (unsigned)(1024 + (fj & 7) * 128 + (fj & 8) * 2 + (lane & 3) * 4);
    O_ADDR(vw, rw, 0); O_ADDR(vs, rs, 1);
    unsigned fp = (unsigned)(size_t)shm_raw + FLG + (wid & 3) * 320 + 256;
    asm volatile("" : "+v"(fp));
    unsigned va[12];
#pragma unroll
    for (int i = 0; i < 12; i++) { va[i] = i < 8 ? vw + i * 8 * (N_ * 2) : vs + (i - 8) * 16 * (N_ * 2); asm volatile("" : "+v"(va[i])); }
  if (wid < 6) {
    QRegs qa, q; u32x2 sa, sb;
    Q_SC(sa, k1);
    Q_SC(sb, k2);
    BAR();
    ISSUE_X(0, 0);
    SIGNAL(0x1000u);
    Q_ROWS(qa, Q_SO(k1));
    WAIT_V(8);
    SIGNAL(1u);
    { const char* b_ = xbase + k1 * 128; const unsigned l_ = ldsx + SB; X8(0, l_, b_); X8(8, l_ + 8192, b_); }
    SIGNAL(16u);
    asm volatile("" :: "v"(ct));
    Q_ROWS(q, Q_SO(k2));
    ROUND(qa, sa, SB + XB + kc0 * KCB, 24, 1);
    WAIT_V(8);
    WAIT_L(0);
    BAR();
    X8(0, ldsx + 2 * SB, xbase + k2 * 128);
    ROUND(q, sb, STG_OFF + kc0 * KCB, 8, 1);
    X8(8, ldsx + 2 * SB + 8192, xbase + k2 * 128);
    WAIT_L(0);
    unsigned long long o = ord >> 12; unsigned l_ = ldsx;
#pragma unroll 1
    for (int t = 0; t < NT - 3; t++) {
      const char* b_ = xbase + ((unsigned)o & 15) * 128; unsigned l2 = l_ + 4096;
      asm volatile("" : "+s"(b_), "+s"(l_), "+s"(l2));
      if (t) { WAIT_V(16); BAR(); }
      XJ(0, 8, l_, b_); XJ(4, 8, l2, b_);
      o >>= 4; l_ = l_ == ldsx + 2 * SB ? ldsx : l_ + SB;
    }
    WAIT_V(16); BAR();
    WAIT_V(8); BAR();
    WAIT_V(0);
  } else {
    QRegs q, qa; u32x2 sa, sb; unsigned tagv;
    Q_SC(sa, k1); Q_SC(sb, k2); TAG_LOAD(tagv);
    BAR();
    AWAIT(0xf000u, 0x2000u);
    Q_ROWS(qa, Q_SO(k1));
    ROUND(qa, sa, SB + XB + kc0 * KCB, 0, 1);
    AWAIT(0xf0u, 32u);
    Q_ROWS(q, Q_SO(k2));
    asm volatile("" :: "v"(ct));
    WAIT_L(0);
    asm volatile("" : "+v"(tagv));
    const unsigned tag = TAG_OF(tagv);
    unsigned fv = tagv;
    const unsigned fvo6 = wid == 6 ? fvo : 0u;
    BAR();
    asm volatile("global_load_dword %0, %1, %2 sc1" : "+v"(fv) : "v"(fvo6), "s"(flags) : "memory");
    ROUND(q, sb, STG_OFF + kc0 * KCB, 1, 1);
    WAIT_L(0);
    const char* wbase = ws + (size_t)tn * NT * WB;
    const unsigned wv = (unsigned)(lane * 16), wv2 = wv + 4096, wv3 = wv + 8192, wv4 = wv + 12288;
    const unsigned ldsw = (unsigned)(size_t)(shm) + XB;
#define W4(v_, l_, b_) asm volatile("s_mov_b32 m0, %1\n\ts_nop 0\n\t"                                                             \
                   "global_load_lds_dwordx4 %0, %2 sc1\n\tglobal_load_lds_dwordx4 %0, %2 offset:1024 sc1\n\t"                     \
                   "global_load_lds_dwordx4 %0, %2 offset:2048 sc1\n\tglobal_load_lds_dwordx4 %0, %2 offset:3072 sc1" :: "v"(v_), "s"(l_), "s"(b_) : "memory")
#define W2(v_, l_, b_) asm volatile("s_mov_b32 m0, %1\n\ts_nop 0\n\t"                                                             \
                   "global_load_lds_dwordx4 %0, %2 sc1\n\tglobal_load_lds_dwordx4 %0, %2 offset:1024 sc1" :: "v"(v_), "s"(l_), "s"(b_) : "memory")
#define W1(v_, o_, l_, b_) asm volatile("s_mov_b32 m0, %1\n\ts_nop 0\n\tglobal_load_lds_dwordx4 %0, %2 offset:%3 sc1" :: "v"(v_), "s"(l_), "s"(b_), "n"(o_) : "memory")
   if (__builtin_expect(wid == 6, 1)) {
    asm volatile(
      "s_lshr_b64 s[64:65], %[ord], 12\n\ts_mov_b32 s66, 0\n\ts_mov_b32 s67, 3\n\ts_mov_b32 s69, %[l0]\n\t"
      "s_mov_b32 s70, 0\n\ts_mov_b32 s71, 0\n\ts_mov_b32 m0, s69\n\ts_and_b32 s60, s64, 15\n\ts_mulk_i32 s60, 0x3800\n\t"
      "s_add_u32 s60, s60, %[blo]\n\ts_addc_u32 s61, %[bhi], 0\n\ts_waitcnt vmcnt(0)\n\t"
      "v_cmp_ne_u32_e64 s[62:63], %[fv], %[tag]\n\ts_bitset1_b32 s63, 20\n\ts_ff1_i32_b64 s68, s[62:63]\n\t"
      "s_lshr_b32 s68, s68, 2\n\ts_add_i32 s68, s68, 2\n\ts_mov_b64 vcc, 0\n\ts_cmp_gt_i32 s67, s68\n\t"
      "s_cbranch_scc1 .Lw%=\n\ts_branch .Lf%=\n\t"
      ".La%=:\n\ts_waitcnt vmcnt(14)\n\t"
      ".Lb%=:\n\ts_waitcnt vmcnt(15)\n\t"
      "s_barrier\n\ts_cbranch_scc1 .Ls%=\n\ts_sleep 5\n\t"
      ".Lf%=:\n\tglobal_load_lds_dwordx4 %[w0], s[60:61] sc1\n\t"
      "global_load_lds_dwordx4 %[w0], s[60:61] offset:1024 sc1\n\t"
      "global_load_lds_dwordx4 %[w0], s[60:61] offset:2048 sc1\n\t"
      "global_load_lds_dwordx4 %[w0], s[60:61] offset:3072 sc1\n\ts_cbranch_vccz .Lg%=\n\ts_waitcnt vmcnt(14)\n\t"
      "v_cmp_ne_u32_e64 s[62:63], %[fv], %[tag]\n\ts_bitset1_b32 s63, 20\n\ts_ff1_i32_b64 s68, s[62:63]\n\t"
      "s_lshr_b32 s68, s68, 2\n\ts_add_i32 s68, s68, 2\n\t"
      ".Lg%=:\n\ts_add_i32 s62, s66, 5\n\ts_min_i32 s62, s62, 15\n\ts_cmp_lt_i32 s68, s62\n\ts_cselect_b64 vcc, -1, 0\n\t"
      "s_cbranch_scc0 .Lh%=\n\tglobal_load_dword %[fv], %[fvo], %[fl] sc1\n\t"
      ".Lh%=:\n\ts_add_u32 m0, m0, 0x1000\n\ts_nop 0\n\tglobal_load_lds_dwordx4 %[w1], s[60:61] sc1\n\t"
      "global_load_lds_dwordx4 %[w1], s[60:61] offset:1024 sc1\n\t"
      "global_load_lds_dwordx4 %[w1], s[60:61] offset:2048 sc1\n\t"
      "global_load_lds_dwordx4 %[w1], s[60:61] offset:3072 sc1\n\ts_add_u32 m0, m0, 0x1000\n\ts_nop 0\n\t"
      "global_load_lds_dwordx4 %[w2], s[60:61] sc1\n\tglobal_load_lds_dwordx4 %[w2], s[60:61] offset:1024 sc1\n\t"
      "global_load_lds_dwordx4 %[w2], s[60:61] offset:2048 sc1\n\t"
      "global_load_lds_dwordx4 %[w2], s[60:61] offset:3072 sc1\n\ts_add_u32 m0, m0, 0x1000\n\ts_nop 0\n\t"
      "global_load_lds_dwordx4 %[w3], s[60:61] sc1\n\tglobal_load_lds_dwordx4 %[w3], s[60:61] offset:1024 sc1\n\t"
      "s_add_i32 s67, s67, 1\n\ts_lshr_b64 s[64:65], s[64:65], 4\n\ts_and_b32 s60, s64, 15\n\ts_mulk_i32 s60, 0x3800\n\t"
      "s_add_u32 s60, s60, %[blo]\n\ts_addc_u32 s61, %[bhi], 0\n\ts_add_i32 s69, s69, 0xb800\n\t"
      "s_cmp_gt_u32 s69, %[lmax]\n\ts_cselect_b32 s69, %[l0], s69\n\ts_mov_b32 m0, s69\n\ts_add_i32 s66, s66, 1\n\t"
      "s_mov_b32 s70, 1\n\ts_cmp_gt_i32 s67, 14\n\ts_cbranch_scc1 .Lx%=\n\ts_cmp_gt_i32 s67, s68\n\t"
      "s_cbranch_vccz .La%=\n\ts_branch .Lb%=\n\t"
      ".Ls%=:\n\ts_cmp_lg_u32 s70, 0\n\ts_cbranch_scc1 .Lt%=\n\ts_waitcnt vmcnt(0)\n\t"
      ".Lt%=:\n\ts_waitcnt vmcnt(10)\n\tv_cmp_ne_u32_e64 s[62:63], %[fv], %[tag]\n\ts_bitset1_b32 s63, 20\n\t"
      "s_ff1_i32_b64 s68, s[62:63]\n\ts_lshr_b32 s68, s68, 2\n\ts_add_i32 s68, s68, 2\n\ts_add_i32 s62, s66, 2\n\t"
      "s_cmp_gt_i32 s67, s62\n\ts_cbranch_scc1 .Lw%=\n\t"
      ".Lu%=:\n\ts_cmp_le_i32 s67, s68\n\ts_cbranch_scc1 .Lv%=\n\ts_add_i32 s71, s71, 1\n\ts_mov_b32 s68, 16\n\t"
      "s_cmp_gt_u32 s71, %[smax]\n\ts_cbranch_scc1 .Lv%=\n\tglobal_load_dword %[fv], %[fvo], %[fl] sc1\n\t"
      "s_waitcnt vmcnt(0)\n\tv_cmp_ne_u32_e64 s[62:63], %[fv], %[tag]\n\ts_bitset1_b32 s63, 20\n\t"
      "s_ff1_i32_b64 s68, s[62:63]\n\ts_lshr_b32 s68, s68, 2\n\ts_add_i32 s68, s68, 2\n\ts_branch .Lu%=\n\t"
      ".Lv%=:\n\tglobal_load_lds_dwordx4 %[w0], s[60:61] sc1\n\t"
      "global_load_lds_dwordx4 %[w0], s[60:61] offset:1024 sc1\n\t"
      "global_load_lds_dwordx4 %[w0], s[60:61] offset:2048 sc1\n\t"
      "global_load_lds_dwordx4 %[w0], s[60:61] offset:3072 sc1\n\ts_add_u32 m0, m0, 0x1000\n\ts_nop 0\n\t"
      "global_load_lds_dwordx4 %[w1], s[60:61] sc1\n\tglobal_load_lds_dwordx4 %[w1], s[60:61] offset:1024 sc1\n\t"
      "global_load_lds_dwordx4 %[w1], s[60:61] offset:2048 sc1\n\t"
      "global_load_lds_dwordx4 %[w1], s[60:61] offset:3072 sc1\n\ts_add_u32 m0, m0, 0x1000\n\ts_nop 0\n\t"
      "global_load_lds_dwordx4 %[w2], s[60:61] sc1\n\tglobal_load_lds_dwordx4 %[w2], s[60:61] offset:1024 sc1\n\t"
      "global_load_lds_dwordx4 %[w2], s[60:61] offset:2048 sc1\n\t"
      "global_load_lds_dwordx4 %[w2], s[60:61] offset:3072 sc1\n\ts_add_u32 m0, m0, 0x1000\n\ts_nop 0\n\t"
      "global_load_lds_dwordx4 %[w3], s[60:61] sc1\n\tglobal_load_lds_dwordx4 %[w3], s[60:61] offset:1024 sc1\n\t"
      "s_add_i32 s67, s67, 1\n\ts_lshr_b64 s[64:65], s[64:65], 4\n\ts_and_b32 s60, s64, 15\n\ts_mulk_i32 s60, 0x3800\n\t"
      "s_add_u32 s60, s60, %[blo]\n\ts_addc_u32 s61, %[bhi], 0\n\ts_add_i32 s69, s69, 0xb800\n\t"
      "s_cmp_gt_u32 s69, %[lmax]\n\ts_cselect_b32 s69, %[l0], s69\n\ts_mov_b32 m0, s69\n\ts_cmp_gt_i32 s67, 14\n\t"
      "s_cbranch_scc1 .Ly%=\n\t"
      ".Lw%=:\n\ts_mov_b64 vcc, 0\n\ts_cmp_le_i32 s67, s68\n\ts_cbranch_scc1 .Lf%=\n\t"
      "global_load_dword %[fv], %[fvo], %[fl] sc1\n\ts_add_i32 s66, s66, 1\n\ts_mov_b32 s70, 0\n\ts_cmp_eq_u32 s70, 0\n\t"
      "s_waitcnt vmcnt(1)\n\ts_branch .Lb%=\n\t"
      ".Lx%=:\n\ts_waitcnt vmcnt(14)\n\t"
      "s_barrier\n\t"
      ".Ly%=:\n\ts_add_i32 s66, s66, 1\n\ts_cmp_lt_u32 s66, 14\n\ts_waitcnt vmcnt(0)\n\t"
      "s_barrier\n\ts_cbranch_scc1 .Ly%=\n\t"
      : [fv] "+v"(fv)
      : [tag] "v"(tag), [fvo] "v"(fvo), [w0] "v"(wv), [w1] "v"(wv2), [w2] "v"(wv3), [w3] "v"(wv4), [fl] "s"(flags), [ord] "s"(ord),
        [blo] "s"((unsigned)(size_t)wbase), [bhi] "s"((unsigned)((size_t)wbase >> 32)), [l0] "s"(ldsw), [lmax] "s"(ldsw + 2 * SB), [smax] "s"(SPIN_MAX)
      : "memory", "scc", "vcc", "s60", "s61", "s62", "s63", "s64", "s65", "s66", "s67", "s68", "s69", "s70", "s71");
   } else {
#define SET_RDY(f_) do { const unsigned long long b_ = __ballot((f_) != tag);                                                     \
      rdy = 2 + ((int)__builtin_ctzll(b_ | (1ull << 52)) >> 2); } while (0)
#define POLL_BLOCK() do { unsigned f_; asm volatile("global_load_dword %0, %1, %2 sc1\n\ts_waitcnt vmcnt(0)" : "=&v"(f_) : "v"(fvo), "s"(flags) : "memory"); \
      SET_RDY(f_); } while (0)
    const char* b7 = wbase + KS(NT - 1) * WB; const unsigned l7 = (unsigned)(size_t)(shm) + STG_OFF;
    BAR();
#pragma unroll 1
    for (int j = 0; j < 2; j++) PUBLISH(STG_OFF + 2 * j * KCB, k2, j);
    BAR();
#pragma unroll 1
    for (int j = 2; j < 4; j++) PUBLISH(STG_OFF + 2 * j * KCB, k2, j);
    BAR();
    WAIT_V(0);
    if (lane < 4) __hip_atomic_store(flags + 256 + tm * 32 + 4 + lane, tag, RLX_AGENT);
    BAR();
    BAR();
    BAR();
    BAR();
    asm volatile("global_load_dword %0, %1, %2 sc1" : "+v"(fv) : "v"(fvo), "s"(flags) : "memory");
    BAR();
    WAIT_V(0);
    asm volatile("" : "+v"(fv));
    int rdy; SET_RDY(fv);
    const bool ok = rdy == NT - 1;
    if (ok && tm == 0 && lane == 0) __hip_atomic_store(flags, tag, RLX_AGENT);
    if (ok) { __builtin_amdgcn_s_sleep(6); W1(wv, 0, l7 + 0, b7); W1(wv, 1024, l7 + 0, b7); W1(wv, 2048, l7 + 0, b7); W1(wv, 3072, l7 + 0, b7); }
    BAR();
    if (ok) { __builtin_amdgcn_s_sleep(6); W1(wv2, 0, l7 + 4096, b7); W1(wv2, 1024, l7 + 4096, b7); W1(wv2, 2048, l7 + 4096, b7); W1(wv2, 3072, l7 + 4096, b7); }
    BAR();
    if (ok) { __builtin_amdgcn_s_sleep(6); W1(wv3, 0, l7 + 8192, b7); W1(wv3, 1024, l7 + 8192, b7); W1(wv3, 2048, l7 + 8192, b7); W1(wv3, 3072, l7 + 8192, b7); }
    BAR();
    if (ok) { __builtin_amdgcn_s_sleep(6); W1(wv4, 0, l7 + 12288, b7); W1(wv4, 1024, l7 + 12288, b7); }
    BAR();
    if (__builtin_expect(!ok, 0)) {
      unsigned spins = 0;
#pragma unroll 1
      while (rdy < NT - 1) { if (++spins > SPIN_MAX) break; POLL_BLOCK(); }
      W4(wv, l7, b7); W4(wv2, l7 + 4096, b7); W4(wv3, l7 + 8192, b7); W2(wv4, l7 + 12288, b7);
      if (tm == 0 && lane == 0) __hip_atomic_store(flags, tag, RLX_AGENT);
    }
    WAIT_V(0);
    BAR();
    BAR();
   }
  }
#define UNIT(u_, a0_, o0_, a1_, o1_) u32x4 p0_, p1_; do { unsigned f_;                                                                 \
      do { asm volatile("ds_read_b32 %0, %3 offset:%8\n\tds_read_b128 %1, %4 offset:%6\n\tds_read_b128 %2, %5 offset:%7\n\ts_waitcnt lgkmcnt(0)" \
                        : "=&v"(f_), "=&v"(p0_), "=&v"(p1_) : "v"(fp), "v"(a0_), "v"(a1_), "n"(o0_), "n"(o1_), "n"(4 * (u_)) : "memory");        \
      } while (__builtin_amdgcn_readfirstlane(f_) == 0); } while (0)
#define UNIT1(u_, a0_, o0_) u32x4 p0_; do { unsigned f_;                                                                              \
      do { asm volatile("ds_read_b32 %0, %2 offset:%5\n\tds_read_b128 %1, %3 offset:%4\n\ts_waitcnt lgkmcnt(0)"                             \
                        : "=&v"(f_), "=&v"(p0_) : "v"(fp), "v"(a0_), "n"(o0_), "n"(4 * (u_)) : "memory");                                        \
      } while (__builtin_amdgcn_readfirstlane(f_) == 0); } while (0)
    { BAR(); const u32x4 p0_ = O_RD(rw, 0), p1_ = O_RD(rw, 8); O_ST(p0_, va[0], 0, AUX_NT); O_ST(p1_, va[1], 0, AUX_NT); }
    { BAR(); const u32x4 p0_ = O_RD(rw, 16), p1_ = O_RD(rw, 24); O_ST(p0_, va[2], 0, AUX_NT); O_ST(p1_, va[3], 0, AUX_NT); }
    { BAR(); const u32x4 p0_ = O_RD(rw, 32), p1_ = O_RD(rw, 40); O_ST(p0_, va[4], 0, AUX_NT); O_ST(p1_, va[5], 0, AUX_NT); }
    { UNIT(3, rw, 6144, rw, 7168); O_ST(p0_, va[6], 0, AUX_NT); O_ST(p1_, va[7], 0, AUX_NT); }
    { UNIT1(4, rs, 0); O_ST(p0_, va[8], 0, AUX_NT); }
    { UNIT1(5, rs, 2048); O_ST(p0_, va[9], 0, AUX_NT); }
    { UNIT1(6, rs, 4096); O_ST(p0_, va[10], 0, AUX_NT); }
    { UNIT1(7, rs, 6144); O_ST(p0_, va[11], 0, AUX_NT); }
  }
  asm volatile("" ::: "memory");
}

extern "C" void kernel_launch(void* const* d_in, const int* in_sizes, int n_in, void* d_out, int out_size, void* d_ws,
                              size_t ws_size, hipStream_t stream) {
  nf4_gemm<<<256, 512, 0, stream>>>((const uint16_t*)d_in[0], (const int*)d_in[1], (const uint16_t*)d_in[2],
                                    (const uint16_t*)d_in[3], (char*)d_ws, (uint16_t*)d_out);
}
```
